```python
import torch

def _softmax(x, axis=-1):
    return torch.softmax(x, dim=axis)
def _gelu(x):
    return torch.nn.functional.gelu(x, approximate="tanh")
def _silu(x):
    return torch.nn.functional.silu(x)
def _sigmoid(x):
    return torch.sigmoid(x)
def _layernorm(x, axis=-1, eps=1e-5):
    m = x.mean(dim=axis, keepdim=True)
    v = x.var(dim=axis, keepdim=True, unbiased=False)
    return (x - m) / torch.sqrt(v + eps)
def _scatter_add(tgt, idx, src, axis=0):
    return tgt.clone().index_add_(axis, idx, src)
def _segment_sum(src, seg, n):
    out = torch.zeros((n,) + src.shape[1:], dtype=src.dtype)
    return out.index_add_(0, seg, src)
def _pad(x, pw):
    flat = tuple(v for pair in reversed(pw) for v in pair)
    return torch.nn.functional.pad(x, flat)

kernel_name = 'standalone__glu_gated_swiglu__sampling'

def setup_inputs():

    _w = (lambda a: (lambda t: t.to(torch.bfloat16) if t.is_floating_point() else t)(torch.as_tensor(a)))
    B, S, D, F = 16, 32, 1024, 3584
    return {
        "x": _w((lambda *s: torch.randn(*s, dtype=torch.bfloat16) * 0.05)(B, S, D)),
        "w1": _w((lambda *s: torch.randn(*s, dtype=torch.bfloat16) * 0.05)(D, F) * (2.0 / D) ** 0.5),
        "w3": _w((lambda *s: torch.randn(*s, dtype=torch.bfloat16) * 0.05)(D, F) * (2.0 / D) ** 0.5),
        "w2": _w((lambda *s: torch.randn(*s, dtype=torch.bfloat16) * 0.05)(F, D) * (2.0 / F) ** 0.5),
    }

def reference(x, w1, w3, w2):
    gate = torch.einsum("bsd,df->bsf", x, w1)
    up = torch.einsum("bsd,df->bsf", x, w3)
    h = _silu(gate) * up
    out = torch.einsum("bsf,fd->bsd", h, w2)
    return out
```

```cpp
#include <hip/hip_runtime.h>
#include <hip/hip_bf16.h>
typedef short bf16x8 __attribute__((ext_vector_type(8)));
typedef float f32x4 __attribute__((ext_vector_type(4)));
typedef unsigned short u16;
constexpr int M = 512, D = 1024, F = 3584;

__device__ __forceinline__ u16 f2bf(float f) { __hip_bfloat16 b = __float2bfloat16(f); return *(u16*)&b; }

__global__ __launch_bounds__(256) void k1(const u16* x, const u16* w1, const u16* w3, u16* h) {
  int wave = blockIdx.x * 4 + (threadIdx.x >> 6), lane = threadIdx.x & 63;
  int tn = wave % (F / 16), tm = wave / (F / 16);
  int r = lane & 15, q = lane >> 4;
  f32x4 g = {0, 0, 0, 0}, u = {0, 0, 0, 0};
  for (int k0 = 0; k0 < D; k0 += 32) {
    bf16x8 a = *(const bf16x8*)(x + (tm * 16 + r) * D + k0 + q * 8);
    bf16x8 b1, b3;
    for (int j = 0; j < 8; ++j) {
      b1[j] = w1[(k0 + q * 8 + j) * F + tn * 16 + r];
      b3[j] = w3[(k0 + q * 8 + j) * F + tn * 16 + r];
    }
    g = __builtin_amdgcn_mfma_f32_16x16x32_bf16(a, b1, g, 0, 0, 0);
    u = __builtin_amdgcn_mfma_f32_16x16x32_bf16(a, b3, u, 0, 0, 0);
  }
  for (int j = 0; j < 4; ++j) {
    float gv = g[j], s = gv / (1.f + __expf(-gv));
    h[(tm * 16 + q * 4 + j) * F + tn * 16 + r] = f2bf(s * u[j]);
  }
}
__global__ __launch_bounds__(256) void k2(const u16* h, const u16* w2, u16* out) {
  int wave = blockIdx.x * 4 + (threadIdx.x >> 6), lane = threadIdx.x & 63;
  int tn = wave % (D / 16), tm = wave / (D / 16);
  int r = lane & 15, q = lane >> 4;
  f32x4 c = {0, 0, 0, 0};
  for (int k0 = 0; k0 < F; k0 += 32) {
    bf16x8 a = *(const bf16x8*)(h + (tm * 16 + r) * F + k0 + q * 8);
    bf16x8 b;
    for (int j = 0; j < 8; ++j) b[j] = w2[(k0 + q * 8 + j) * D + tn * 16 + r];
    c = __builtin_amdgcn_mfma_f32_16x16x32_bf16(a, b, c, 0, 0, 0);
  }
  for (int j = 0; j < 4; ++j) out[(tm * 16 + q * 4 + j) * D + tn * 16 + r] = f2bf(c[j]);
}
extern "C" void kernel_launch(void* const* d_in, const int* in_sizes, int n_in, void* d_out, int out_size, void* d_ws,
                              size_t ws_size, hipStream_t stream) {
  const u16* x = (const u16*)d_in[0];
  const u16* w1 = (const u16*)d_in[1];
  const u16* w3 = (const u16*)d_in[2];
  const u16* w2 = (const u16*)d_in[3];
  u16* h = (u16*)d_ws;
  k1<<<(M / 16) * (F / 16) / 4, 256, 0, stream>>>(x, w1, w3, h);
  k2<<<(M / 16) * (D / 16) / 4, 256, 0, stream>>>(h, w2, (u16*)d_out);
}
```

```cpp
#include <hip/hip_runtime.h>
#include <hip/hip_bf16.h>
typedef short bf16x8 __attribute__((ext_vector_type(8)));
typedef float f32x4 __attribute__((ext_vector_type(4)));
typedef unsigned short u16;
constexpr int M = 512, D = 1024, F = 3584;

__device__ __forceinline__ u16 f2bf(float f) { __hip_bfloat16 b = __float2bfloat16(f); return *(u16*)&b; }

__global__ __launch_bounds__(256) void k1(const u16* x, const u16* w1, const u16* w3, u16* h) {
  int wave = blockIdx.x * 4 + (threadIdx.x >> 6), lane = threadIdx.x & 63;
  int tn = wave % (F / 16), tm = wave / (F / 16);
  int r = lane & 15, q = lane >> 4;
  f32x4 g = {0, 0, 0, 0}, u = {0, 0, 0, 0};
  for (int k0 = 0; k0 < D; k0 += 32) {
    bf16x8 a = *(const bf16x8*)(x + (tm * 16 + r) * D + k0 + q * 8);
    bf16x8 b1, b3;
    for (int j = 0; j < 8; ++j) {
      b1[j] = w1[(k0 + q * 8 + j) * F + tn * 16 + r];
      b3[j] = w3[(k0 + q * 8 + j) * F + tn * 16 + r];
    }
    g = __builtin_amdgcn_mfma_f32_16x16x32_bf16(a, b1, g, 0, 0, 0);
    u = __builtin_amdgcn_mfma_f32_16x16x32_bf16(a, b3, u, 0, 0, 0);
  }
  for (int j = 0; j < 4; ++j) {
    float gv = g[j], s = gv / (1.f + __expf(-gv));
    h[(tm * 16 + q * 4 + j) * F + tn * 16 + r] = f2bf(s * u[j]);
  }
}
__global__ __launch_bounds__(256) void k2(const u16* h, const u16* w2, u16* out) {
  int wave = blockIdx.x * 4 + (threadIdx.x >> 6), lane = threadIdx.x & 63;
  int tn = wave % (D / 16), tm = wave / (D / 16);
  int r = lane & 15, q = lane >> 4;
  f32x4 c = {0, 0, 0, 0};
  for (int k0 = 0; k0 < F; k0 += 32) {
    bf16x8 a = *(const bf16x8*)(h + (tm * 16 + r) * F + k0 + q * 8);
    bf16x8 b;
    for (int j = 0; j < 8; ++j) b[j] = w2[(k0 + q * 8 + j) * D + tn * 16 + r];
    c = __builtin_amdgcn_mfma_f32_16x16x32_bf16(a, b, c, 0, 0, 0);
  }
  for (int j = 0; j < 4; ++j) out[(tm * 16 + q * 4 + j) * D + tn * 16 + r] = f2bf(c[j]);
}

typedef unsigned long long u64;
typedef unsigned u32x4 __attribute__((ext_vector_type(4)));
__device__ __forceinline__ void dma(unsigned m0, unsigned voff, __amdgpu_buffer_rsrc_t r, unsigned soff) {
  asm volatile("s_mov_b32 m0, %0\n\ts_nop 0\n\tbuffer_load_dwordx4 %1, %2, %3 offen lds" ::"s"(m0), "v"(voff), "s"(r), "s"(soff) : "memory");
}
__device__ __forceinline__ void spin(u64 ticks) {
  u64 t0 = __builtin_amdgcn_s_memrealtime();
  while (__builtin_amdgcn_s_memrealtime() - t0 < ticks) __builtin_amdgcn_s_sleep(8);
}
__global__ __launch_bounds__(256) void probeA(const void* x, const void* w1, const void* w3) {
  __shared__ char lds[65536];
  const int bid = blockIdx.x, xcd = bid & 7, idx = bid >> 3;
  const int wv = __builtin_amdgcn_readfirstlane(threadIdx.x >> 6), lane = threadIdx.x & 63;
  u64 r0 = __builtin_amdgcn_s_memrealtime();
  __amdgpu_buffer_rsrc_t rx = __builtin_amdgcn_make_buffer_rsrc((void*)x, 0, M * D * 2, 0x00020000);
  __amdgpu_buffer_rsrc_t ra = __builtin_amdgcn_make_buffer_rsrc((void*)w1, 0, D * F * 2, 0x00020000);
  __amdgpu_buffer_rsrc_t rb = __builtin_amdgcn_make_buffer_rsrc((void*)w3, 0, D * F * 2, 0x00020000);
  asm volatile("" :: "v"(&lds[0]));
  if (idx < 28) {
    const int mt = idx & 3, ft = xcd * 7 + (idx >> 2) % 7;
    unsigned vx = (mt * 128 + wv * 32 + (lane >> 3)) * (D * 2) + (lane & 7) * 16;
    const int wh = wv & 1;
    unsigned vw = (wh * 32 + (lane >> 3)) * (F * 2) + ft * 128 + (lane & 7) * 16;
    __amdgpu_buffer_rsrc_t rw = wv < 2 ? ra : rb;
#pragma unroll 1
    for (int t = 0; t < 16; ++t) {
      const unsigned slot = (t & 1) * 32768 + wv * 8192;
#pragma unroll
      for (int i = 0; i < 4; ++i) dma(slot + i * 1024, vx, rx, t * 128 + i * 8 * (D * 2));
#pragma unroll
      for (int i = 0; i < 4; ++i) dma(slot + (4 + i) * 1024, vw, rw, t * 64 * (F * 2) + i * 8 * (F * 2));
      asm volatile("s_waitcnt vmcnt(16)" ::: "memory");
      __builtin_amdgcn_s_barrier();
    }
    asm volatile("s_waitcnt vmcnt(0)" ::: "memory");
  }
  __builtin_amdgcn_s_barrier();
  u64 r1 = __builtin_amdgcn_s_memrealtime();
  if (bid == 0) spin(200 * (r1 - r0));
}
__global__ __launch_bounds__(64) void probeB(const void* p, int bytes, int start, int stride, int n) {
  __amdgpu_buffer_rsrc_t r = __builtin_amdgcn_make_buffer_rsrc((void*)p, 0, bytes, 0x00020000);
  u64 r0 = __builtin_amdgcn_s_memrealtime();
  unsigned off = start, d;
#pragma unroll 1
  for (int i = 0; i < n; ++i) {
    asm volatile("buffer_load_dword %0, %1, %2, 0 offen\n\ts_waitcnt vmcnt(0)" : "=&v"(d) : "v"(off), "s"(r) : "memory");
    off += stride;
  }
  u64 r1 = __builtin_amdgcn_s_memrealtime();
  spin(200 * (r1 - r0));
}
extern "C" void kernel_launch(void* const* d_in, const int* in_sizes, int n_in, void* d_out, int out_size, void* d_ws,
                              size_t ws_size, hipStream_t stream) {
  const u16* x = (const u16*)d_in[0];
  const u16* w1 = (const u16*)d_in[1];
  const u16* w3 = (const u16*)d_in[2];
  const u16* w2 = (const u16*)d_in[3];
  u16* h = (u16*)d_ws;
  probeB<<<1,64,0,stream>>>(w1, D*F*2, 0, 4096+128, 1);
  k1<<<(M / 16) * (F / 16) / 4, 256, 0, stream>>>(x, w1, w3, h);
  k2<<<(M / 16) * (D / 16) / 4, 256, 0, stream>>>(h, w2, (u16*)d_out);
}
```

```cpp
#include <hip/hip_runtime.h>
#include <hip/hip_bf16.h>
typedef short bf16x8 __attribute__((ext_vector_type(8)));
typedef float f32x4 __attribute__((ext_vector_type(4)));
typedef unsigned short u16;
constexpr int M = 512, D = 1024, F = 3584;

__device__ __forceinline__ u16 f2bf(float f) { __hip_bfloat16 b = __float2bfloat16(f); return *(u16*)&b; }

__global__ __launch_bounds__(256) void k1(const u16* x, const u16* w1, const u16* w3, u16* h) {
  int wave = blockIdx.x * 4 + (threadIdx.x >> 6), lane = threadIdx.x & 63;
  int tn = wave % (F / 16), tm = wave / (F / 16);
  int r = lane & 15, q = lane >> 4;
  f32x4 g = {0, 0, 0, 0}, u = {0, 0, 0, 0};
  for (int k0 = 0; k0 < D; k0 += 32) {
    bf16x8 a = *(const bf16x8*)(x + (tm * 16 + r) * D + k0 + q * 8);
    bf16x8 b1, b3;
    for (int j = 0; j < 8; ++j) {
      b1[j] = w1[(k0 + q * 8 + j) * F + tn * 16 + r];
      b3[j] = w3[(k0 + q * 8 + j) * F + tn * 16 + r];
    }
    g = __builtin_amdgcn_mfma_f32_16x16x32_bf16(a, b1, g, 0, 0, 0);
    u = __builtin_amdgcn_mfma_f32_16x16x32_bf16(a, b3, u, 0, 0, 0);
  }
  for (int j = 0; j < 4; ++j) {
    float gv = g[j], s = gv / (1.f + __expf(-gv));
    h[(tm * 16 + q * 4 + j) * F + tn * 16 + r] = f2bf(s * u[j]);
  }
}
__global__ __launch_bounds__(256) void k2(const u16* h, const u16* w2, u16* out) {
  int wave = blockIdx.x * 4 + (threadIdx.x >> 6), lane = threadIdx.x & 63;
  int tn = wave % (D / 16), tm = wave / (D / 16);
  int r = lane & 15, q = lane >> 4;
  f32x4 c = {0, 0, 0, 0};
  for (int k0 = 0; k0 < F; k0 += 32) {
    bf16x8 a = *(const bf16x8*)(h + (tm * 16 + r) * F + k0 + q * 8);
    bf16x8 b;
    for (int j = 0; j < 8; ++j) b[j] = w2[(k0 + q * 8 + j) * D + tn * 16 + r];
    c = __builtin_amdgcn_mfma_f32_16x16x32_bf16(a, b, c, 0, 0, 0);
  }
  for (int j = 0; j < 4; ++j) out[(tm * 16 + q * 4 + j) * D + tn * 16 + r] = f2bf(c[j]);
}

typedef unsigned long long u64;
typedef unsigned u32x4 __attribute__((ext_vector_type(4)));
__device__ __forceinline__ void spin(u64 ticks) {
  u64 t0 = __builtin_amdgcn_s_memrealtime();
  while (__builtin_amdgcn_s_memrealtime() - t0 < ticks) __builtin_amdgcn_s_sleep(8);
}
__device__ __forceinline__ u64 rt() { u64 t; asm volatile("s_memrealtime %0\n\ts_waitcnt lgkmcnt(0)" : "=s"(t)::"memory"); return t; }
template <int NT, int PASS2>
__global__ __launch_bounds__(256) void probeC(const void* w1, const void* w3, const void* w2) {
  __shared__ char lds[98304];
  asm volatile("" :: "v"(&lds[0]));
  const int bid = blockIdx.x;
  const int wv = __builtin_amdgcn_readfirstlane(threadIdx.x >> 6), lane = threadIdx.x & 63;
  __amdgpu_buffer_rsrc_t ra = __builtin_amdgcn_make_buffer_rsrc((void*)w1, 0, D * F * 2, 0x00020000);
  __amdgpu_buffer_rsrc_t rb = __builtin_amdgcn_make_buffer_rsrc((void*)w3, 0, D * F * 2, 0x00020000);
  __amdgpu_buffer_rsrc_t rc = __builtin_amdgcn_make_buffer_rsrc((void*)w2, 0, D * F * 2, 0x00020000);
  asm volatile("" :: "s"(ra), "s"(rb), "s"(rc));
  u64 r0 = rt();
  u32x4 d;
  const unsigned vo = bid * 28672 + wv * 1024 + lane * 16;
#pragma unroll 1
  for (int i = 0; i < 7; ++i) {
    if (NT) {
      asm volatile("buffer_load_dwordx4 %0, %1, %2, %3 offen nt" : "=&v"(d) : "v"(vo), "s"(ra), "s"(i * 4096) : "memory");
      asm volatile("buffer_load_dwordx4 %0, %1, %2, %3 offen nt" : "=&v"(d) : "v"(vo), "s"(rb), "s"(i * 4096) : "memory");
      asm volatile("buffer_load_dwordx4 %0, %1, %2, %3 offen nt" : "=&v"(d) : "v"(vo), "s"(rc), "s"(i * 4096) : "memory");
    } else {
      asm volatile("buffer_load_dwordx4 %0, %1, %2, %3 offen" : "=&v"(d) : "v"(vo), "s"(ra), "s"(i * 4096) : "memory");
      asm volatile("buffer_load_dwordx4 %0, %1, %2, %3 offen" : "=&v"(d) : "v"(vo), "s"(rb), "s"(i * 4096) : "memory");
      asm volatile("buffer_load_dwordx4 %0, %1, %2, %3 offen" : "=&v"(d) : "v"(vo), "s"(rc), "s"(i * 4096) : "memory");
    }
  }
  asm volatile("s_waitcnt vmcnt(0)" ::: "memory");
  __builtin_amdgcn_s_barrier();
  u64 r1 = rt();
  if (PASS2) {
    spin(500);
    __builtin_amdgcn_s_barrier();
    r0 = rt();
#pragma unroll 1
    for (int i = 0; i < 7; ++i) {
      asm volatile("buffer_load_dwordx4 %0, %1, %2, %3 offen" : "=&v"(d) : "v"(vo), "s"(ra), "s"(i * 4096) : "memory");
      asm volatile("buffer_load_dwordx4 %0, %1, %2, %3 offen" : "=&v"(d) : "v"(vo), "s"(rb), "s"(i * 4096) : "memory");
      asm volatile("buffer_load_dwordx4 %0, %1, %2, %3 offen" : "=&v"(d) : "v"(vo), "s"(rc), "s"(i * 4096) : "memory");
    }
    asm volatile("s_waitcnt vmcnt(0)" ::: "memory");
    __builtin_amdgcn_s_barrier();
    r1 = rt();
  }
  spin(200 * (r1 - r0));
}
template <int KB>
__global__ __launch_bounds__(256) void probeI() {
  __shared__ char lds[98304];
  asm volatile("" :: "v"(&lds[0]));
  u64 r0 = rt();
  asm volatile(".rept %0\n\ts_nop 0\n\t.endr" :: "n"(KB * 256));
  u64 r1 = rt();
  spin(200 * (r1 - r0));
}
__global__ __launch_bounds__(256) void probeG1(u64* t) { u64 r = rt(); if (threadIdx.x == 0) t[blockIdx.x] = r; }
__global__ __launch_bounds__(256) void probeG2(const u64* t) {
  u64 r = rt();
  u64 v = t[threadIdx.x];
  for (int o = 32; o; o >>= 1) { u64 w = __shfl_xor(v, o); v = w > v ? w : v; }
  __shared__ u64 s[4];
  s[threadIdx.x >> 6] = v;
  __syncthreads();
  u64 m = s[0]; for (int i = 1; i < 4; ++i) m = s[i] > m ? s[i] : m;
  u64 dt = r - m;
  if (dt > 5000) dt = 5000;
  spin(200 * dt);
}
extern "C" void kernel_launch(void* const* d_in, const int* in_sizes, int n_in, void* d_out, int out_size, void* d_ws,
                              size_t ws_size, hipStream_t stream) {
  const u16* x = (const u16*)d_in[0];
  const u16* w1 = (const u16*)d_in[1];
  const u16* w3 = (const u16*)d_in[2];
  const u16* w2 = (const u16*)d_in[3];
  u16* h = (u16*)d_ws;
  probeI<4><<<256,256,0,stream>>>();
  k1<<<(M / 16) * (F / 16) / 4, 256, 0, stream>>>(x, w1, w3, h);
  k2<<<(M / 16) * (D / 16) / 4, 256, 0, stream>>>(h, w2, (u16*)d_out);
}
```

```cpp
#include <hip/hip_runtime.h>

struct Ord {
  unsigned long long o[8];
};

__attribute__((aligned(4096))) __global__ __launch_bounds__(512, 2) void swiglu_fused(const void* x, const void* w1, const void* w3, const void* w2,
                                                       void* out, void* ws, Ord ord) {
  __shared__ __attribute__((aligned(1024))) char lds[163840];
  asm volatile(
      "s_load_dwordx8 s[36:43], %0, 0\n\t"
      "s_load_dwordx4 s[44:47], %0, 32\n\t"
      "s_and_b32 s12, %1, 7\n\t"
      "s_bfe_u32 s13, %1, 0x20003\n\t"
      "s_lshr_b32 s14, %1, 5\n\t"
      "s_add_u32 s5, s12, 3\n\t"
      "s_and_b32 s5, s5, 7\n\t"
      "s_add_u32 s64, s13, s14\n\t"
      "s_cmp_gt_u32 s14, s5\n\t"
      "s_subb_u32 s64, s64, 0\n\t"
      "s_and_b32 s80, s64, 3\n\t"
      "s_cmp_eq_u32 s14, s5\n\t"
      "s_cselect_b32 s65, 4, 0\n\t"
      "s_or_b32 s80, s80, s65\n\t"
      "s_lshl3_add_u32 s64, s80, 48\n\t"
      "s_load_dwordx2 s[8:9], %0, s64\n\t"
      "v_lshrrev_b32 v4, 6, %2\n\t"
      "v_and_b32 v1, 63, %2\n\t"
      "s_mul_i32 s15, s12, 7\n\t"
      "v_readfirstlane_b32 s16, v4\n\t"
      "s_add_u32 s15, s15, s14\n\t"
      "s_and_b32 s17, s16, 3\n\t"
      "s_cmp_ge_u32 s16, 4\n\t"
      "s_cbranch_scc1 .L_SETUP_%=\n\t"
      "s_setprio 3\n\t"
      "s_cmp_eq_u32 s14, 7\n\t"
      "s_cbranch_scc1 .L_SETUP_%=\n\t"

      "v_lshrrev_b32 v4, 3, v1\n\t"
      "v_lshrrev_b32 v5, 4, v1\n\t"
      "v_and_b32 v6, 7, v1\n\t"
      "v_xor_b32 v7, v6, v5\n\t"
      "s_lshl2_add_u32 s64, s13, s17\n\t"
      "s_lshl_b32 s64, s64, 16\n\t"
      "v_lshlrev_b32 v7, 4, v7\n\t"
      "v_lshl_or_b32 v7, v4, 11, v7\n\t"
      "v_or_b32 v140, s64, v7\n\t"
      "v_xor_b32 v141, 64, v140\n\t"
      "v_add_u32 v142, 30720, v140\n\t"
      "v_add_u32 v143, 46080, v141\n\t"
      "v_add_u32 v141, 15360, v141\n\t"
      "v_and_b32 v5, 2, v4\n\t"
      "v_xor_b32 v6, v6, v5\n\t"
      "v_lshlrev_b32 v6, 4, v6\n\t"
      "s_and_b32 s64, s17, 1\n\t"
      "s_mul_i32 s64, s64, 229376\n\t"
      "s_lshl_b32 s65, s15, 7\n\t"
      "s_add_u32 s64, s64, s65\n\t"
      "v_mul_u32_u24 v144, 7168, v4\n\t"
      "v_add3_u32 v144, v144, v6, s64\n\t"
      "v_xor_b32 v145, 64, v144\n\t"
      "v_add_u32 v146, 112640, v144\n\t"
      "v_add_u32 v147, 168960, v145\n\t"
      "v_add_u32 v145, 56320, v145\n\t"
      "s_lshl_b32 s69, s17, 12\n\t"
      "s_mov_b32 m0, s69\n\t"
      "s_mov_b32 s50, 0x100000\n\t"
      "s_mov_b32 s51, 0x20000\n\t"
      "s_mov_b32 s54, 0x700000\n\t"
      "s_mov_b32 s55, 0x20000\n\t"
      "s_waitcnt lgkmcnt(0)\n\t"
      "s_mov_b32 s48, s36\n\t"
      "s_and_b32 s49, s37, 0xffff\n\t"
      "s_and_b32 s64, s8, 15\n\t"
      "s_lshl_b32 s70, s64, 7\n\t"
      "buffer_load_dwordx4 v140, s[48:51], s70 offen offset:0 lds\n\t"
      "s_mul_i32 s71, s64, 458752\n\t"
      "s_cmp_lt_u32 s17, 2\n\t"
      "s_cselect_b64 s[52:53], s[38:39], s[40:41]\n\t"
      "buffer_load_dwordx4 v141, s[48:51], s70 offen offset:1024 lds\n\t"
      "s_and_b32 s53, s53, 0xffff\n\t"
      "buffer_load_dwordx4 v142, s[48:51], s70 offen offset:2048 lds\n\t"
      "buffer_load_dwordx4 v143, s[48:51], s70 offen offset:3072 lds\n\t"
      "s_add_u32 m0, s69, 16384\n\t"
      "s_lshr_b64 s[8:9], s[8:9], 4\n\t"
      "buffer_load_dwordx4 v144, s[52:55], s71 offen offset:0 lds\n\t"
      "buffer_load_dwordx4 v145, s[52:55], s71 offen offset:1024 lds\n\t"
      "buffer_load_dwordx4 v146, s[52:55], s71 offen offset:2048 lds\n\t"
      "buffer_load_dwordx4 v147, s[52:55], s71 offen offset:3072 lds\n\t"

      ".L_SETUP_%=:\n\t"
      "s_lshr_b32 s18, s12, 2\n\t"
      "s_xor_b32 s18, s18, 1\n\t"
      "s_lshr_b32 s64, s14, 1\n\t"
      "s_add_u32 s64, s64, s13\n\t"
      "s_and_b32 s64, s64, 3\n\t"
      "s_and_b32 s19, s12, 3\n\t"
      "s_lshl2_add_u32 s19, s19, s64\n\t"
      "s_and_b32 s64, s14, 1\n\t"
      "s_lshl1_add_u32 s20, s13, s64\n\t"
      "s_lshl2_add_u32 s21, s18, s17\n\t"
      "s_lshl_b32 s21, s21, 1\n\t"
      "s_mul_i32 s86, s21, 458752\n\t"
      "s_lshl_b32 s65, s19, 7\n\t"
      "s_add_u32 s86, s86, s65\n\t"
      "s_add_u32 s67, s17, 1\n\t"
      "s_and_b32 s67, s67, 3\n\t"
      "s_lshl2_add_u32 s67, s18, s67\n\t"
      "s_lshl1_add_u32 s67, s67, 1\n\t"
      "s_mul_i32 s26, s67, 458752\n\t"
      "s_add_u32 s26, s26, s65\n\t"
      "s_mul_i32 s87, s17, 24576\n\t"
      "s_lshl_b32 s66, s17, 14\n\t"
      "s_add_u32 s66, s66, 98304\n\t"
      "v_lshrrev_b32 v4, 3, v1\n\t"
      "v_and_b32 v5, 6, v4\n\t"
      "v_and_b32 v6, 7, v1\n\t"
      "v_xor_b32 v5, v5, v6\n\t"
      "v_lshlrev_b32 v5, 4, v5\n\t"
      "v_lshl_or_b32 v244, v4, 11, v5\n\t"
      "v_add_u32 v245, 15360, v244\n\t"
      "v_add_u32 v246, 30720, v244\n\t"
      "v_add_u32 v247, 46080, v244\n\t"
      "s_lshl3_add_u32 s24, s13, s12\n\t"
      "s_lshl3_add_u32 s24, s24, s14\n\t"
      "s_cmp_lt_u32 s16, 4\n\t"
      "s_cselect_b32 s25, s66, s87\n\t"
      "s_cselect_b32 s21, s67, s21\n\t"
      "s_lshl4_add_u32 s64, s20, s21\n\t"
      "s_mul_i32 s64, s64, 896\n\t"
      "s_add_u32 s64, s64, 0x380000\n\t"
      "v_lshl_add_u32 v14, v1, 7, s64\n\t"
      "s_waitcnt lgkmcnt(0)\n\t"
      "s_mov_b32 s28, s42\n\t"
      "s_and_b32 s29, s43, 0xffff\n\t"
      "s_mov_b32 s30, 0x700000\n\t"
      "s_mov_b32 s31, 0x20000\n\t"
      "s_mov_b32 s56, s46\n\t"
      "s_and_b32 s57, s47, 0xffff\n\t"
      "s_mov_b32 s58, 0x5b0000\n\t"
      "s_mov_b32 s59, 0x20000\n\t"
      "s_mov_b32 s60, s44\n\t"
      "s_and_b32 s61, s45, 0xffff\n\t"
      "s_mov_b32 s62, 0x100000\n\t"
      "s_mov_b32 s63, 0x20000\n\t"
      "v_lshlrev_b32 v3, 4, v1\n\t"
      "v_lshrrev_b32 v4, 4, v1\n\t"
      "v_bfe_u32 v5, v1, 2, 2\n\t"
      "v_bfe_u32 v6, v1, 3, 2\n\t"
      "v_lshlrev_b32 v5, 7, v5\n\t"
      "v_lshl_or_b32 v5, v6, 5, v5\n\t"
      "v_and_b32 v6, 3, v1\n\t"
      "v_lshl_or_b32 v5, v6, 3, v5\n\t"
      "v_lshl_or_b32 v20, v4, 10, v5\n\t"
      "v_lshl_or_b32 v252, v4, 9, v5\n\t"
      "v_add_u32 v252, s25, v252\n\t"
      "v_xor_b32 v253, 32, v252\n\t"
      "v_xor_b32 v254, 64, v252\n\t"
      "v_xor_b32 v255, 0x60, v252\n\t"
      "s_lshl1_add_u32 s64, s20, s18\n\t"
      "s_lshl_b32 s10, s64, 16\n\t"
      "s_lshl_b32 s65, s19, 7\n\t"
      "s_add_u32 s10, s10, s65\n\t"
      "s_mul_i32 s65, s21, 7168\n\t"
      "s_mul_i32 s76, s64, 229376\n\t"
      "s_xor_b32 s64, s64, 1\n\t"
      "s_mul_i32 s72, s64, 229376\n\t"
      "s_add_u32 s72, s72, s65\n\t"
      "s_add_u32 s76, s76, s65\n\t"
      "s_add_u32 s74, s72, 114688\n\t"
      "s_add_u32 s78, s76, 114688\n\t"
      "s_add_u32 s73, s72, 4096\n\t"
      "s_add_u32 s75, s74, 4096\n\t"
      "s_add_u32 s77, s76, 4096\n\t"
      "s_add_u32 s79, s78, 4096\n\t"
      "v_and_b32 v5, 15, v1\n\t"
      "v_and_b32 v6, 7, v1\n\t"
      "v_xor_b32 v6, v4, v6\n\t"
      "v_lshlrev_b32 v6, 4, v6\n\t"
      "v_lshl_or_b32 v6, v5, 8, v6\n\t"
      "v_add_u32 v22, s25, v6\n\t"
      "v_xor_b32 v23, 64, v22\n\t"
      "v_lshl_add_u32 v7, s16, 2, v4\n\t"
      "v_and_b32 v6, 7, v7\n\t"
      "v_xor_b32 v6, v6, v5\n\t"
      "v_lshlrev_b32 v6, 4, v6\n\t"
      "v_lshl_or_b32 v248, v7, 8, v6\n\t"
      "v_lshlrev_b32 v6, 3, v5\n\t"
      "v_lshl_or_b32 v251, v7, 11, v6\n\t"
      "v_add_u32 v249, 73728, v248\n\t"
      "v_add_u32 v250, 131072, v248\n\t"
      "s_lshl3_add_u32 s64, s24, s16\n\t"
      "s_lshl_b32 s64, s64, 10\n\t"
      "s_add_u32 s94, s64, 0x3b0000\n\t"
      "s_xor_b32 s64, s64, 0x40000\n\t"
      "s_add_u32 s95, s64, 0x3b0000\n\t"
      "s_lshr_b32 s64, s17, 1\n\t"
      "s_lshl1_add_u32 s64, s13, s64\n\t"
      "s_and_b32 s65, s17, 1\n\t"
      "s_lshl1_add_u32 s65, s15, s65\n\t"
      "s_mul_i32 s66, s64, 112\n\t"
      "s_lshl2_add_u32 s6, s66, s65\n\t"
      "s_lshl_b32 s6, s6, 10\n\t"
      "s_add_u32 s11, s66, s65\n\t"
      "s_mov_b32 s22, 0\n\t"
      "s_mov_b32 s27, 1\n\t"
      "s_movk_i32 s4, 0xff\n\t"
      "s_mov_b32 s23, 0\n\t"
      "s_cmp_ge_u32 s16, 4\n\t"
      "s_cbranch_scc1 .L_CONS_%=\n\t"

      "v_mov_b32 v208, 0xffbfffff\n\t"
      "v_mov_b32 v209, 0xffbfffff\n\t"
      "v_mov_b32 v210, 0xffbfffff\n\t"
      "v_mov_b32 v211, 0xffbfffff\n\t"
      "s_lshl_b32 s80, s80, 2\n\t"
      "s_lshr_b32 s80, 0x2434c694, s80\n\t"
      "s_and_b32 s80, s80, 15\n\t"
      "s_add_u32 s67, s69, 98304\n\t"
      "s_mov_b32 s68, 1\n\t"
      "s_add_u32 s69, s69, 32768\n\t"
      "s_and_b32 s64, s8, 15\n\t"
      "s_mov_b32 m0, s69\n\t"
      "s_lshl_b32 s70, s64, 7\n\t"
      "s_lshl_b32 s65, s13, 7\n\t"
      "s_add_u32 s65, s65, 0x3a4000\n\t"
      "buffer_load_dwordx2 v[12:13], off, s[56:59], s65 sc1\n\t"
      "s_cmp_eq_u32 s14, 7\n\t"
      "s_cbranch_scc1 .L_HELPL_%=\n\t"
      "s_branch .L_LA_%=\n\t"
      ".L_FILL_%=:\n\t"
      "buffer_store_dwordx4 v[208:211], v3, s[56:59], s6 offen sc1\n\t"
      "s_add_u32 s6, s6, 114688\n\t"
      "buffer_store_dwordx4 v[208:211], v3, s[56:59], s6 offen sc1\n\t"
      "s_add_u32 s6, s6, 114688\n\t"
      "buffer_store_dwordx4 v[208:211], v3, s[56:59], s6 offen sc1\n\t"
      "s_add_u32 s6, s6, 114688\n\t"
      "buffer_store_dwordx4 v[208:211], v3, s[56:59], s6 offen sc1\n\t"
      "s_branch .L_FILLED_%=\n\t"
      ".L_LTOP_%=:\n\t"
      "buffer_load_dwordx4 v146, s[52:55], s71 offen offset:2048 lds\n\t"
      "buffer_load_dwordx4 v147, s[52:55], s71 offen offset:3072 lds\n\t"
      "s_mov_b32 m0, s69\n\t"
      "s_waitcnt vmcnt(8)\n\t"
      "s_barrier\n\t"
      ".L_LA_%=:\n\t"
      "buffer_load_dwordx4 v140, s[48:51], s70 offen offset:0 lds\n\t"
      "s_mul_i32 s71, s64, 458752\n\t"
      "s_cmp_eq_u32 s68, s80\n\t"
      "s_cbranch_scc1 .L_FILL_%=\n\t"
      ".L_FILLED_%=:\n\t"
      "buffer_load_dwordx4 v141, s[48:51], s70 offen offset:1024 lds\n\t"
      "s_lshr_b64 s[8:9], s[8:9], 4\n\t"
      "buffer_load_dwordx4 v142, s[48:51], s70 offen offset:2048 lds\n\t"
      "s_and_b32 s64, s8, 15\n\t"
      "buffer_load_dwordx4 v143, s[48:51], s70 offen offset:3072 lds\n\t"
      "s_add_u32 m0, s69, 16384\n\t"
      "s_lshl_b32 s70, s64, 7\n\t"
      "buffer_load_dwordx4 v144, s[52:55], s71 offen offset:0 lds\n\t"
      "s_add_u32 s69, s69, 32768\n\t"
      "s_cmp_eq_u32 s69, s67\n\t"
      "s_cselect_b32 s65, 98304, 0\n\t"
      "s_sub_u32 s69, s69, s65\n\t"
      "buffer_load_dwordx4 v145, s[52:55], s71 offen offset:1024 lds\n\t"
      "s_add_u32 s68, s68, 1\n\t"
      "s_cmp_lt_u32 s68, 16\n\t"
      "s_cbranch_scc1 .L_LTOP_%=\n\t"
      "buffer_load_dwordx4 v146, s[52:55], s71 offen offset:2048 lds\n\t"
      "buffer_load_dwordx4 v147, s[52:55], s71 offen offset:3072 lds\n\t"
      "s_waitcnt vmcnt(8)\n\t"
      "s_barrier\n\t"

      ".L_STAGE_%=:\n\t"
      "s_add_u32 m0, s25, 0\n\t"
      "s_nop 0\n\t"
      "buffer_load_dwordx4 v244, s[28:31], s26 offen offset:0 lds\n\t"
      "buffer_load_dwordx4 v245, s[28:31], s26 offen offset:1024 lds\n\t"
      "buffer_load_dwordx4 v246, s[28:31], s26 offen offset:2048 lds\n\t"
      "buffer_load_dwordx4 v247, s[28:31], s26 offen offset:3072 lds\n\t"
      "s_add_u32 s26, s26, 65536\n\t"
      "s_add_u32 m0, s25, 4096\n\t"
      "s_nop 0\n\t"
      "buffer_load_dwordx4 v244, s[28:31], s26 offen offset:0 lds\n\t"
      "buffer_load_dwordx4 v245, s[28:31], s26 offen offset:1024 lds\n\t"
      "buffer_load_dwordx4 v246, s[28:31], s26 offen offset:2048 lds\n\t"
      "buffer_load_dwordx4 v247, s[28:31], s26 offen offset:3072 lds\n\t"
      "s_add_u32 s26, s26, 65536\n\t"
      "s_waitcnt vmcnt(8)\n\t"
      "s_barrier\n\t"
      "s_setprio 0\n\t"
      "s_add_u32 m0, s25, 8192\n\t"
      "s_nop 0\n\t"
      "buffer_load_dwordx4 v244, s[28:31], s26 offen offset:0 lds\n\t"
      "buffer_load_dwordx4 v245, s[28:31], s26 offen offset:1024 lds\n\t"
      "buffer_load_dwordx4 v246, s[28:31], s26 offen offset:2048 lds\n\t"
      "buffer_load_dwordx4 v247, s[28:31], s26 offen offset:3072 lds\n\t"
      "s_add_u32 s26, s26, 65536\n\t"
      "s_add_u32 m0, s25, 12288\n\t"
      "s_nop 0\n\t"
      "buffer_load_dwordx4 v244, s[28:31], s26 offen offset:0 lds\n\t"
      "buffer_load_dwordx4 v245, s[28:31], s26 offen offset:1024 lds\n\t"
      "buffer_load_dwordx4 v246, s[28:31], s26 offen offset:2048 lds\n\t"
      "buffer_load_dwordx4 v247, s[28:31], s26 offen offset:3072 lds\n\t"
      "s_add_u32 s26, s26, 65536\n\t"
      "s_barrier\n\t"
      "s_waitcnt vmcnt(8)\n\t"
      "s_add_u32 m0, s87, 0\n\t"
      "s_nop 0\n\t"
      "buffer_load_dwordx4 v244, s[28:31], s86 offen offset:0 lds\n\t"
      "ds_read_b64_tr_b16 v[24:25], v252 offset:0\n\t"
      "ds_read_b64_tr_b16 v[26:27], v252 offset:2048\n\t"
      "buffer_load_dwordx4 v245, s[28:31], s86 offen offset:1024 lds\n\t"
      "ds_read_b64_tr_b16 v[28:29], v253 offset:0\n\t"
      "ds_read_b64_tr_b16 v[30:31], v253 offset:2048\n\t"
      "buffer_load_dwordx4 v246, s[28:31], s86 offen offset:2048 lds\n\t"
      "ds_read_b64_tr_b16 v[32:33], v254 offset:0\n\t"
      "ds_read_b64_tr_b16 v[34:35], v254 offset:2048\n\t"
      "buffer_load_dwordx4 v247, s[28:31], s86 offen offset:3072 lds\n\t"
      "ds_read_b64_tr_b16 v[36:37], v255 offset:0\n\t"
      "ds_read_b64_tr_b16 v[38:39], v255 offset:2048\n\t"
      "s_add_u32 s86, s86, 65536\n\t"
      "s_add_u32 m0, s87, 4096\n\t"
      "s_nop 0\n\t"
      "buffer_load_dwordx4 v244, s[28:31], s86 offen offset:0 lds\n\t"
      "ds_read_b64_tr_b16 v[40:41], v252 offset:4096\n\t"
      "ds_read_b64_tr_b16 v[42:43], v252 offset:6144\n\t"
      "buffer_load_dwordx4 v245, s[28:31], s86 offen offset:1024 lds\n\t"
      "ds_read_b64_tr_b16 v[44:45], v253 offset:4096\n\t"
      "ds_read_b64_tr_b16 v[46:47], v253 offset:6144\n\t"
      "buffer_load_dwordx4 v246, s[28:31], s86 offen offset:2048 lds\n\t"
      "ds_read_b64_tr_b16 v[48:49], v254 offset:4096\n\t"
      "ds_read_b64_tr_b16 v[50:51], v254 offset:6144\n\t"
      "buffer_load_dwordx4 v247, s[28:31], s86 offen offset:3072 lds\n\t"
      "ds_read_b64_tr_b16 v[52:53], v255 offset:4096\n\t"
      "ds_read_b64_tr_b16 v[54:55], v255 offset:6144\n\t"
      "s_add_u32 s86, s86, 65536\n\t"
      "s_waitcnt vmcnt(8)\n\t"
      "s_add_u32 m0, s87, 8192\n\t"
      "s_nop 0\n\t"
      "buffer_load_dwordx4 v244, s[28:31], s86 offen offset:0 lds\n\t"
      "ds_read_b64_tr_b16 v[56:57], v252 offset:8192\n\t"
      "ds_read_b64_tr_b16 v[58:59], v252 offset:10240\n\t"
      "buffer_load_dwordx4 v245, s[28:31], s86 offen offset:1024 lds\n\t"
      "ds_read_b64_tr_b16 v[60:61], v253 offset:8192\n\t"
      "ds_read_b64_tr_b16 v[62:63], v253 offset:10240\n\t"
      "buffer_load_dwordx4 v246, s[28:31], s86 offen offset:2048 lds\n\t"
      "ds_read_b64_tr_b16 v[64:65], v254 offset:8192\n\t"
      "ds_read_b64_tr_b16 v[66:67], v254 offset:10240\n\t"
      "buffer_load_dwordx4 v247, s[28:31], s86 offen offset:3072 lds\n\t"
      "ds_read_b64_tr_b16 v[68:69], v255 offset:8192\n\t"
      "ds_read_b64_tr_b16 v[70:71], v255 offset:10240\n\t"
      "s_add_u32 s86, s86, 65536\n\t"
      "s_add_u32 m0, s87, 12288\n\t"
      "s_nop 0\n\t"
      "buffer_load_dwordx4 v244, s[28:31], s86 offen offset:0 lds\n\t"
      "ds_read_b64_tr_b16 v[72:73], v252 offset:12288\n\t"
      "ds_read_b64_tr_b16 v[74:75], v252 offset:14336\n\t"
      "buffer_load_dwordx4 v245, s[28:31], s86 offen offset:1024 lds\n\t"
      "ds_read_b64_tr_b16 v[76:77], v253 offset:12288\n\t"
      "ds_read_b64_tr_b16 v[78:79], v253 offset:14336\n\t"
      "buffer_load_dwordx4 v246, s[28:31], s86 offen offset:2048 lds\n\t"
      "ds_read_b64_tr_b16 v[80:81], v254 offset:12288\n\t"
      "ds_read_b64_tr_b16 v[82:83], v254 offset:14336\n\t"
      "buffer_load_dwordx4 v247, s[28:31], s86 offen offset:3072 lds\n\t"
      "ds_read_b64_tr_b16 v[84:85], v255 offset:12288\n\t"
      "ds_read_b64_tr_b16 v[86:87], v255 offset:14336\n\t"
      "s_add_u32 s86, s86, 65536\n\t"
      "s_add_u32 m0, s87, 16384\n\t"
      "s_nop 0\n\t"
      "buffer_load_dwordx4 v244, s[28:31], s86 offen offset:0 lds\n\t"
      "buffer_load_dwordx4 v245, s[28:31], s86 offen offset:1024 lds\n\t"
      "buffer_load_dwordx4 v246, s[28:31], s86 offen offset:2048 lds\n\t"
      "buffer_load_dwordx4 v247, s[28:31], s86 offen offset:3072 lds\n\t"
      "s_add_u32 s86, s86, 65536\n\t"
      "v_lshl_add_u32 v12, v12, 2, v12\n\t"
      "v_lshl_add_u32 v13, v13, 5, v13\n\t"
      "v_add_u32 v12, 0xac564b05, v12\n\t"
      "v_add_u32 v13, 0x9e3779b9, v13\n\t"
      "s_add_u32 m0, s87, 20480\n\t"
      "s_nop 0\n\t"
      "buffer_load_dwordx4 v244, s[28:31], s86 offen offset:0 lds\n\t"
      "buffer_load_dwordx4 v245, s[28:31], s86 offen offset:1024 lds\n\t"
      "buffer_load_dwordx4 v246, s[28:31], s86 offen offset:2048 lds\n\t"
      "buffer_load_dwordx4 v247, s[28:31], s86 offen offset:3072 lds\n\t"
      "s_add_u32 s86, s86, 65536\n\t"
      "s_waitcnt lgkmcnt(0)\n\t"
      "s_add_u32 m0, s25, 12288\n\t"
      "s_nop 0\n\t"
      "buffer_load_dwordx4 v244, s[28:31], s86 offen offset:0 lds\n\t"
      "buffer_load_dwordx4 v245, s[28:31], s86 offen offset:1024 lds\n\t"
      "buffer_load_dwordx4 v246, s[28:31], s86 offen offset:2048 lds\n\t"
      "buffer_load_dwordx4 v247, s[28:31], s86 offen offset:3072 lds\n\t"
      "s_add_u32 s86, s86, 65536\n\t"
      "s_add_u32 m0, s25, 0\n\t"
      "s_nop 0\n\t"
      "buffer_load_dwordx4 v244, s[28:31], s26 offen offset:0 lds\n\t"
      "buffer_load_dwordx4 v245, s[28:31], s26 offen offset:1024 lds\n\t"
      "buffer_load_dwordx4 v246, s[28:31], s26 offen offset:2048 lds\n\t"
      "buffer_load_dwordx4 v247, s[28:31], s26 offen offset:3072 lds\n\t"
      "s_add_u32 s26, s26, 65536\n\t"
      "s_waitcnt vmcnt(4)\n\t"
      "s_barrier\n\t"
      "s_add_u32 m0, s25, 4096\n\t"
      "s_nop 0\n\t"
      "buffer_load_dwordx4 v244, s[28:31], s26 offen offset:0 lds\n\t"
      "buffer_load_dwordx4 v245, s[28:31], s26 offen offset:1024 lds\n\t"
      "buffer_load_dwordx4 v246, s[28:31], s26 offen offset:2048 lds\n\t"
      "buffer_load_dwordx4 v247, s[28:31], s26 offen offset:3072 lds\n\t"
      "s_add_u32 s26, s26, 65536\n\t"
      "s_mov_b64 exec, 0x7f\n\t"
      "buffer_load_dwordx2 v[16:17], v14, s[56:59], 0 offen sc1\n\t"
      "s_mov_b64 exec, -1\n\t"
      "s_add_u32 m0, s25, 8192\n\t"
      "s_nop 0\n\t"
      "buffer_load_dwordx4 v244, s[28:31], s26 offen offset:0 lds\n\t"
      "buffer_load_dwordx4 v245, s[28:31], s26 offen offset:1024 lds\n\t"
      "buffer_load_dwordx4 v246, s[28:31], s26 offen offset:2048 lds\n\t"
      "buffer_load_dwordx4 v247, s[28:31], s26 offen offset:3072 lds\n\t"
      "s_add_u32 s26, s26, 65536\n\t"
      "s_waitcnt vmcnt(8)\n\t"
      "ds_read_b64_tr_b16 v[88:89], v252 offset:0\n\t"
      "ds_read_b64_tr_b16 v[90:91], v252 offset:2048\n\t"
      "ds_read_b64_tr_b16 v[92:93], v253 offset:0\n\t"
      "ds_read_b64_tr_b16 v[94:95], v253 offset:2048\n\t"
      "ds_read_b64_tr_b16 v[96:97], v254 offset:0\n\t"
      "ds_read_b64_tr_b16 v[98:99], v254 offset:2048\n\t"
      "ds_read_b64_tr_b16 v[100:101], v255 offset:0\n\t"
      "ds_read_b64_tr_b16 v[102:103], v255 offset:2048\n\t"
      "s_waitcnt vmcnt(4)\n\t"
      "ds_read_b64_tr_b16 v[104:105], v252 offset:4096\n\t"
      "ds_read_b64_tr_b16 v[106:107], v252 offset:6144\n\t"
      "ds_read_b64_tr_b16 v[108:109], v253 offset:4096\n\t"
      "ds_read_b64_tr_b16 v[110:111], v253 offset:6144\n\t"
      "ds_read_b64_tr_b16 v[112:113], v254 offset:4096\n\t"
      "ds_read_b64_tr_b16 v[114:115], v254 offset:6144\n\t"
      "ds_read_b64_tr_b16 v[116:117], v255 offset:4096\n\t"
      "ds_read_b64_tr_b16 v[118:119], v255 offset:6144\n\t"
      "s_waitcnt vmcnt(0)\n\t"
      "ds_read_b64_tr_b16 v[120:121], v252 offset:8192\n\t"
      "ds_read_b64_tr_b16 v[122:123], v252 offset:10240\n\t"
      "ds_read_b64_tr_b16 v[124:125], v253 offset:8192\n\t"
      "ds_read_b64_tr_b16 v[126:127], v253 offset:10240\n\t"
      "ds_read_b64_tr_b16 v[128:129], v254 offset:8192\n\t"
      "ds_read_b64_tr_b16 v[130:131], v254 offset:10240\n\t"
      "ds_read_b64_tr_b16 v[132:133], v255 offset:8192\n\t"
      "ds_read_b64_tr_b16 v[134:135], v255 offset:10240\n\t"
      "s_branch .L_PLOOP_%=\n\t"

      ".L_REDO0_%=:\n\t"
      "s_add_u32 s23, s23, 1\n\t"
      "s_cmp_gt_u32 s23, 0x100000\n\t"
      "s_cbranch_scc1 .L_KEEP0_%=\n\t"
      "s_waitcnt vmcnt(0)\n\t"
      "buffer_load_dwordx4 v[136:139], v3, s[56:59], s72 offen offset:0 sc1\n\t"
      "buffer_load_dwordx4 v[140:143], v3, s[56:59], s72 offen offset:1024 sc1\n\t"
      "buffer_load_dwordx4 v[144:147], v3, s[56:59], s72 offen offset:2048 sc1\n\t"
      "buffer_load_dwordx4 v[148:151], v3, s[56:59], s72 offen offset:3072 sc1\n\t"
      "buffer_load_dwordx4 v[152:155], v3, s[56:59], s73 offen offset:0 sc1\n\t"
      "buffer_load_dwordx4 v[156:159], v3, s[56:59], s73 offen offset:1024 sc1\n\t"
      "s_waitcnt vmcnt(0)\n\t"
      "s_branch .L_SP0_%=\n\t"
      ".L_REDO1_%=:\n\t"
      "s_add_u32 s23, s23, 1\n\t"
      "s_cmp_gt_u32 s23, 0x100000\n\t"
      "s_cbranch_scc1 .L_KEEP1_%=\n\t"
      "s_waitcnt vmcnt(0)\n\t"
      "buffer_load_dwordx4 v[164:167], v3, s[56:59], s74 offen offset:0 sc1\n\t"
      "buffer_load_dwordx4 v[168:171], v3, s[56:59], s74 offen offset:1024 sc1\n\t"
      "buffer_load_dwordx4 v[172:175], v3, s[56:59], s74 offen offset:2048 sc1\n\t"
      "buffer_load_dwordx4 v[136:139], v3, s[56:59], s74 offen offset:3072 sc1\n\t"
      "buffer_load_dwordx4 v[140:143], v3, s[56:59], s75 offen offset:0 sc1\n\t"
      "buffer_load_dwordx4 v[144:147], v3, s[56:59], s75 offen offset:1024 sc1\n\t"
      "s_waitcnt vmcnt(0)\n\t"
      "s_branch .L_SP1_%=\n\t"
      ".L_REDO2_%=:\n\t"
      "s_add_u32 s23, s23, 1\n\t"
      "s_cmp_gt_u32 s23, 0x100000\n\t"
      "s_cbranch_scc1 .L_KEEP2_%=\n\t"
      "s_waitcnt vmcnt(0)\n\t"
      "buffer_load_dwordx4 v[152:155], v3, s[56:59], s76 offen offset:0 sc1\n\t"
      "buffer_load_dwordx4 v[156:159], v3, s[56:59], s76 offen offset:1024 sc1\n\t"
      "buffer_load_dwordx4 v[160:163], v3, s[56:59], s76 offen offset:2048 sc1\n\t"
      "buffer_load_dwordx4 v[164:167], v3, s[56:59], s76 offen offset:3072 sc1\n\t"
      "buffer_load_dwordx4 v[168:171], v3, s[56:59], s77 offen offset:0 sc1\n\t"
      "buffer_load_dwordx4 v[172:175], v3, s[56:59], s77 offen offset:1024 sc1\n\t"
      "s_waitcnt vmcnt(0)\n\t"
      "s_branch .L_SP2_%=\n\t"
      ".L_REDO3_%=:\n\t"
      "s_add_u32 s23, s23, 1\n\t"
      "s_cmp_gt_u32 s23, 0x100000\n\t"
      "s_cbranch_scc1 .L_KEEP3_%=\n\t"
      "s_waitcnt vmcnt(0)\n\t"
      "buffer_load_dwordx4 v[140:143], v3, s[56:59], s78 offen offset:0 sc1\n\t"
      "buffer_load_dwordx4 v[144:147], v3, s[56:59], s78 offen offset:1024 sc1\n\t"
      "buffer_load_dwordx4 v[148:151], v3, s[56:59], s78 offen offset:2048 sc1\n\t"
      "buffer_load_dwordx4 v[152:155], v3, s[56:59], s78 offen offset:3072 sc1\n\t"
      "buffer_load_dwordx4 v[156:159], v3, s[56:59], s79 offen offset:0 sc1\n\t"
      "buffer_load_dwordx4 v[160:163], v3, s[56:59], s79 offen offset:1024 sc1\n\t"
      "buffer_load_dwordx4 v[164:167], v3, s[56:59], s79 offen offset:2048 sc1\n\t"
      "s_waitcnt vmcnt(0)\n\t"
      "s_branch .L_SP3_%=\n\t"

      ".L_BUMP_%=:\n\t"
      "v_cmp_ne_u64 vcc, v[16:17], v[12:13]\n\t"
      "s_cbranch_vccz .L_BUMPNOW_%=\n\t"
      "s_add_u32 s27, s27, 1\n\t"
      "s_cmp_gt_u32 s27, 0x100000\n\t"
      "s_cbranch_scc1 .L_NOB2_%=\n\t"
      "buffer_load_dwordx2 v[16:17], v15, s[56:59], 0 offen sc1\n\t"
      "s_waitcnt vmcnt(0)\n\t"
      "s_branch .L_BUMP_%=\n\t"
      ".L_BUMPNOW_%=:\n\t"
      "s_lshl_b32 s64, s13, 7\n\t"
      "s_add_u32 s64, s64, 0x3a4000\n\t"
      "s_mov_b64 exec, 1\n\t"
      "buffer_store_dwordx2 v[12:13], off, s[56:59], s64 sc1\n\t"
      "s_mov_b64 exec, -1\n\t"
      "s_branch .L_NOB2_%=\n\t"

      ".L_HELPL_%=:\n\t"
      "s_or_b32 s27, s12, s16\n\t"
      "s_mul_i32 s64, s13, 28672\n\t"
      "s_add_u32 s64, s64, 0x380000\n\t"
      "v_lshl_add_u32 v4, v1, 8, s64\n\t"
      "s_lshl_b32 s64, s13, 13\n\t"
      "s_add_u32 s64, s64, 0x38e000\n\t"
      "v_lshl_add_u32 v5, v1, 10, s64\n\t"
      "v_cmp_gt_u32 vcc, 56, v1\n\t"
      "s_waitcnt vmcnt(0)\n\t"
      "s_barrier\n\t"
      "v_cndmask_b32 v15, v5, v4, vcc\n\t"
      "s_sleep 80\n\t"
      "s_branch .L_STAGE_%=\n\t"

      ".L_HELPC_%=:\n\t"
      "s_lshl_b32 s65, s13, 7\n\t"
      "s_add_u32 s65, s65, 0x3a4000\n\t"
      "buffer_load_dwordx2 v[12:13], off, s[56:59], s65 sc1\n\t"
      "s_and_b32 s11, s24, 0xf8\n\t"
      "s_lshl_b32 s11, s11, 7\n\t"
      "s_add_u32 s11, s11, 0x39c000\n\t"
      "s_cmp_lt_u32 s17, 2\n\t"
      "s_cselect_b64 s[52:53], s[38:39], s[40:41]\n\t"
      "s_and_b32 s53, s53, 0xffff\n\t"
      "s_mov_b32 s54, 0x700000\n\t"
      "s_mov_b32 s55, 0x20000\n\t"
      "v_mul_u32_u24 v4, 7168, v1\n\t"
      "v_lshlrev_b32 v5, 11, v1\n\t"
      "s_lshl_b32 s67, s13, 2\n\t"
      "s_and_b32 s66, s17, 1\n\t"
      "s_mul_i32 s66, s66, 229376\n\t"
      "s_mul_i32 s65, s12, 7\n\t"
      "s_add_u32 s65, s65, s5\n\t"
      "s_lshl_b32 s65, s65, 7\n\t"
      "s_add_u32 s66, s66, s65\n\t"
      "s_waitcnt vmcnt(0)\n\t"
      "v_lshl_add_u32 v12, v12, 2, v12\n\t"
      "v_lshl_add_u32 v13, v13, 5, v13\n\t"
      "v_add_u32 v12, 0xac564b05, v12\n\t"
      "v_add_u32 v13, 0x9e3779b9, v13\n\t"
      "s_barrier\n\t"
      "s_cmp_lg_u32 s17, 0\n\t"
      "s_cbranch_scc1 .L_NOHELLO_%=\n\t"
      "s_mov_b64 exec, 0xffff\n\t"
      "buffer_store_dwordx2 v[12:13], v9, s[56:59], s11 offen sc1\n\t"
      "s_mov_b64 exec, -1\n\t"
      ".L_NOHELLO_%=:\n\t"
      "s_cmp_eq_u32 s5, 7\n\t"
      "s_cbranch_scc1 .L_NOSLOWF_%=\n\t"
      "s_mov_b32 exec_hi, 0\n\t"
      "s_lshr_b32 s64, 0x869f, s67\n\t"
      "s_and_b32 s64, s64, 15\n\t"
      "s_mul_i32 s64, s64, 458752\n\t"
      "s_add_u32 s64, s64, s66\n\t"
      "buffer_load_dword v243, v4, s[52:55], s64 offen\n\t"
      "s_lshr_b32 s64, 0xcab5, s67\n\t"
      "s_and_b32 s64, s64, 15\n\t"
      "s_mul_i32 s64, s64, 458752\n\t"
      "s_add_u32 s64, s64, s66\n\t"
      "buffer_load_dword v243, v4, s[52:55], s64 offen\n\t"
      "s_mov_b64 exec, -1\n\t"
      ".L_NOSLOWF_%=:\n\t"
      "s_bitcmp1_b32 s12, 0\n\t"
      "s_cbranch_scc1 .L_NOSLOWC_%=\n\t"
      "s_lshl2_add_u32 s64, s13, s17\n\t"
      "s_lshl4_add_u32 s64, s18, s64\n\t"
      "s_mul_i32 s64, s64, 229376\n\t"
      "s_or_b32 s65, s19, 3\n\t"
      "s_lshl_b32 s65, s65, 7\n\t"
      "s_add_u32 s64, s64, s65\n\t"
      "s_add_u32 s65, s64, 98304\n\t"
      "s_mov_b32 exec_hi, 0xffff\n\t"
      "buffer_load_dword v243, v5, s[28:31], s64 offen\n\t"
      "s_mov_b64 exec, -1\n\t"
      "buffer_load_dword v243, v5, s[28:31], s65 offen\n\t"
      ".L_NOSLOWC_%=:\n\t"
      "s_barrier\n\t"
      "s_barrier\n\t"
      "s_branch .L_CJOIN_%=\n\t"

      ".p2align 12\n\t"
      ".L_CONS_%=:\n\t"
      "s_lshl_b32 s64, s17, 13\n\t"
      "s_sub_u32 s64, 110592, s64\n\t"
      "v_add_u32 v244, s64, v252\n\t"
      "v_add_u32 v245, s64, v253\n\t"
      "v_add_u32 v246, s64, v254\n\t"
      "v_add_u32 v247, s64, v255\n\t"
      "v_lshlrev_b32 v9, 3, v1\n\t"
      "s_cmp_eq_u32 s14, 7\n\t"
      "s_cbranch_scc1 .L_HELPC_%=\n\t"
      "s_lshl_b32 s11, s11, 7\n\t"
      "s_add_u32 s11, s11, 0x380000\n\t"
      "s_and_b32 s64, s17, 1\n\t"
      "s_lshl_b32 s64, s64, 6\n\t"
      "s_or_b32 s64, s64, 16384\n\t"
      "v_xor_b32 v20, s64, v20\n\t"
      "v_xor_b32 v21, 32, v20\n\t"
      "v_bfe_u32 v6, v1, 1, 3\n\t"
      "v_xor_b32 v4, v4, v6\n\t"
      "v_lshlrev_b32 v5, 7, v5\n\t"
      "v_lshl_or_b32 v4, v4, 4, v5\n\t"
      "s_lshr_b32 s64, s17, 1\n\t"
      "s_lshl_b32 s65, s64, 13\n\t"
      "v_or_b32 v18, s65, v4\n\t"
      "v_xor_b32 v19, 64, v18\n\t"
      "v_mov_b64 v[64:65], 0\n\t"
      "v_mov_b64 v[66:67], 0\n\t"
      "v_mov_b64 v[68:69], 0\n\t"
      "v_mov_b64 v[70:71], 0\n\t"
      "v_mov_b64 v[72:73], 0\n\t"
      "v_mov_b64 v[74:75], 0\n\t"
      "v_mov_b64 v[76:77], 0\n\t"
      "v_mov_b64 v[78:79], 0\n\t"
      "v_mov_b64 v[80:81], 0\n\t"
      "v_mov_b64 v[82:83], 0\n\t"
      "v_mov_b64 v[84:85], 0\n\t"
      "v_mov_b64 v[86:87], 0\n\t"
      "v_mfma_f32_32x32x16_bf16 v[96:111], v[64:67], v[64:67], 0\n\t"
      "v_mfma_f32_32x32x16_bf16 v[112:127], v[64:67], v[64:67], 0\n\t"
      "v_mfma_f32_32x32x16_bf16 v[128:143], v[64:67], v[64:67], 0\n\t"
      "v_mfma_f32_32x32x16_bf16 v[144:159], v[64:67], v[64:67], 0\n\t"
      "s_mov_b32 s80, 0xbfb8aa3b\n\t"
      "s_mov_b32 s81, s80\n\t"
      "s_mov_b32 s82, 1.0\n\t"
      "s_mov_b32 s83, 1.0\n\t"
      "s_mov_b32 s68, 0\n\t"
      "s_mov_b32 s69, 0\n\t"
      "s_setprio 3\n\t"
      "s_lshl_b32 s65, s13, 7\n\t"
      "s_add_u32 s65, s65, 0x3a4000\n\t"
      "buffer_load_dwordx2 v[12:13], off, s[56:59], s65 sc1\n\t"
      "s_setprio 0\n\t"

      "s_branch .L_CBAR_%=\n\t"
      ".p2align 6\n\t"
      ".L_CLOOP_%=:\n\t"
      "v_mfma_f32_16x16x32_bf16 v[136:139], v[60:63], v[80:83], v[136:139]\n\t"
      "v_mfma_f32_16x16x32_bf16 v[132:135], v[60:63], v[76:79], v[132:135]\n\t"
      "v_mfma_f32_16x16x32_bf16 v[128:131], v[60:63], v[72:75], v[128:131]\n\t"
      "s_waitcnt lgkmcnt(0)\n\t"
      ".L_CBAR_%=:\n\t"
      "s_barrier\n\t"
      "v_mfma_f32_16x16x32_bf16 v[112:115], v[64:67], v[72:75], v[112:115]\n\t"
      "ds_read_b128 v[40:43], v18 offset:0\n\t"
      "ds_read_b64_tr_b16 v[24:25], v20 offset:0\n\t"
      "v_mfma_f32_16x16x32_bf16 v[116:119], v[64:67], v[76:79], v[116:119]\n\t"
      "ds_read_b64_tr_b16 v[26:27], v20 offset:512\n\t"
      "ds_read_b128 v[44:47], v18 offset:2048\n\t"
      "v_mfma_f32_16x16x32_bf16 v[120:123], v[64:67], v[80:83], v[120:123]\n\t"
      "ds_read_b128 v[48:51], v18 offset:4096\n\t"
      "ds_read_b128 v[52:55], v18 offset:6144\n\t"
      "v_mfma_f32_16x16x32_bf16 v[124:127], v[64:67], v[84:87], v[124:127]\n\t"
      "ds_read_b64_tr_b16 v[28:29], v20 offset:8192\n\t"
      "v_mfma_f32_16x16x32_bf16 v[156:159], v[68:71], v[84:87], v[156:159]\n\t"
      "ds_read_b64_tr_b16 v[30:31], v20 offset:8704\n\t"
      "v_mfma_f32_16x16x32_bf16 v[152:155], v[68:71], v[80:83], v[152:155]\n\t"
      "ds_read_b64_tr_b16 v[32:33], v21 offset:0\n\t"
      "v_mfma_f32_16x16x32_bf16 v[148:151], v[68:71], v[76:79], v[148:151]\n\t"
      "ds_read_b64_tr_b16 v[34:35], v21 offset:512\n\t"
      "v_mfma_f32_16x16x32_bf16 v[144:147], v[68:71], v[72:75], v[144:147]\n\t"
      "ds_read_b64_tr_b16 v[36:37], v21 offset:8192\n\t"
      "s_waitcnt lgkmcnt(5)\n\t"
      "v_mfma_f32_16x16x32_bf16 v[96:99], v[24:27], v[40:43], v[96:99]\n\t"
      "ds_read_b64_tr_b16 v[38:39], v21 offset:8704\n\t"
      "v_mfma_f32_16x16x32_bf16 v[100:103], v[24:27], v[44:47], v[100:103]\n\t"
      "ds_read_b128 v[72:75], v19 offset:0\n\t"
      "v_mfma_f32_16x16x32_bf16 v[104:107], v[24:27], v[48:51], v[104:107]\n\t"
      "ds_read_b64_tr_b16 v[56:57], v20 offset:4096\n\t"
      "v_mfma_f32_16x16x32_bf16 v[108:111], v[24:27], v[52:55], v[108:111]\n\t"
      "ds_read_b64_tr_b16 v[58:59], v20 offset:4608\n\t"
      "s_waitcnt lgkmcnt(7)\n\t"
      "v_mfma_f32_16x16x32_bf16 v[140:143], v[28:31], v[52:55], v[140:143]\n\t"
      "ds_read_b128 v[76:79], v19 offset:2048\n\t"
      "v_mfma_f32_16x16x32_bf16 v[136:139], v[28:31], v[48:51], v[136:139]\n\t"
      "ds_read_b128 v[80:83], v19 offset:4096\n\t"
      "v_mfma_f32_16x16x32_bf16 v[132:135], v[28:31], v[44:47], v[132:135]\n\t"
      "ds_read_b128 v[84:87], v19 offset:6144\n\t"
      "v_mfma_f32_16x16x32_bf16 v[128:131], v[28:31], v[40:43], v[128:131]\n\t"
      "ds_read_b64_tr_b16 v[60:61], v20 offset:12288\n\t"
      "s_waitcnt lgkmcnt(9)\n\t"
      "v_mfma_f32_16x16x32_bf16 v[112:115], v[32:35], v[40:43], v[112:115]\n\t"
      "ds_read_b64_tr_b16 v[62:63], v20 offset:12800\n\t"
      "v_mfma_f32_16x16x32_bf16 v[116:119], v[32:35], v[44:47], v[116:119]\n\t"
      "ds_read_b64_tr_b16 v[64:65], v21 offset:4096\n\t"
      "v_mfma_f32_16x16x32_bf16 v[120:123], v[32:35], v[48:51], v[120:123]\n\t"
      "ds_read_b64_tr_b16 v[66:67], v21 offset:4608\n\t"
      "v_mfma_f32_16x16x32_bf16 v[124:127], v[32:35], v[52:55], v[124:127]\n\t"
      "ds_read_b64_tr_b16 v[68:69], v21 offset:12288\n\t"
      "s_waitcnt lgkmcnt(11)\n\t"
      "v_mfma_f32_16x16x32_bf16 v[156:159], v[36:39], v[52:55], v[156:159]\n\t"
      "ds_read_b64_tr_b16 v[70:71], v21 offset:12800\n\t"
      "v_mfma_f32_16x16x32_bf16 v[152:155], v[36:39], v[48:51], v[152:155]\n\t"
      "s_add_u32 s69, s69, 1\n\t"
      "s_cmp_eq_u32 s69, 3\n\t"
      "v_mfma_f32_16x16x32_bf16 v[148:151], v[36:39], v[44:47], v[148:151]\n\t"
      "s_cselect_b32 s64, 0xffff0000, s82\n\t"
      "s_cselect_b32 s69, 0, s69\n\t"
      "v_mfma_f32_16x16x32_bf16 v[144:147], v[36:39], v[40:43], v[144:147]\n\t"
      "s_min_i32 s64, s64, 32768\n\t"
      "s_waitcnt lgkmcnt(4)\n\t"
      "v_mfma_f32_16x16x32_bf16 v[96:99], v[56:59], v[72:75], v[96:99]\n\t"
      "v_add_u32 v18, s64, v18\n\t"
      "v_add_u32 v19, s64, v19\n\t"
      "v_mfma_f32_16x16x32_bf16 v[100:103], v[56:59], v[76:79], v[100:103]\n\t"
      "v_add_u32 v20, s64, v20\n\t"
      "v_add_u32 v21, s64, v21\n\t"
      "v_mfma_f32_16x16x32_bf16 v[104:107], v[56:59], v[80:83], v[104:107]\n\t"
      "s_add_u32 s68, s68, 1\n\t"
      "s_cmp_lt_u32 s68, 16\n\t"
      "v_mfma_f32_16x16x32_bf16 v[108:111], v[56:59], v[84:87], v[108:111]\n\t"
      "v_mfma_f32_16x16x32_bf16 v[140:143], v[60:63], v[84:87], v[140:143]\n\t"
      "s_cbranch_scc1 .L_CLOOP_%=\n\t"
      "v_mfma_f32_16x16x32_bf16 v[136:139], v[60:63], v[80:83], v[136:139]\n\t"
      "v_mfma_f32_16x16x32_bf16 v[132:135], v[60:63], v[76:79], v[132:135]\n\t"
      "v_mfma_f32_16x16x32_bf16 v[128:131], v[60:63], v[72:75], v[128:131]\n\t"
      "s_waitcnt vmcnt(0) lgkmcnt(0)\n\t"
      "s_barrier\n\t"
      "s_setprio 3\n\t"
      "v_mfma_f32_16x16x32_bf16 v[112:115], v[64:67], v[72:75], v[112:115]\n\t"
      "v_mfma_f32_16x16x32_bf16 v[144:147], v[68:71], v[72:75], v[144:147]\n\t"
      "v_mfma_f32_16x16x32_bf16 v[116:119], v[64:67], v[76:79], v[116:119]\n\t"
      "v_mfma_f32_16x16x32_bf16 v[148:151], v[68:71], v[76:79], v[148:151]\n\t"
      "v_mfma_f32_16x16x32_bf16 v[120:123], v[64:67], v[80:83], v[120:123]\n\t"
      "v_mfma_f32_16x16x32_bf16 v[152:155], v[68:71], v[80:83], v[152:155]\n\t"
      "v_mfma_f32_16x16x32_bf16 v[124:127], v[64:67], v[84:87], v[124:127]\n\t"
      "v_mfma_f32_16x16x32_bf16 v[156:159], v[68:71], v[84:87], v[156:159]\n\t"
      "v_lshl_add_u32 v12, v12, 2, v12\n\t"
      "v_lshl_add_u32 v13, v13, 5, v13\n\t"
      "v_add_u32 v12, 0xac564b05, v12\n\t"
      "v_add_u32 v13, 0x9e3779b9, v13\n\t"

      "v_pk_mul_f32 v[160:161], v[96:97], s[80:81]\n\t"
      "v_pk_mul_f32 v[162:163], v[98:99], s[80:81]\n\t"
      "v_pk_mul_f32 v[164:165], v[112:113], s[80:81]\n\t"
      "v_pk_mul_f32 v[166:167], v[114:115], s[80:81]\n\t"
      "v_exp_f32 v160, v160\n\t"
      "v_exp_f32 v161, v161\n\t"
      "v_exp_f32 v162, v162\n\t"
      "v_exp_f32 v163, v163\n\t"
      "v_exp_f32 v164, v164\n\t"
      "v_exp_f32 v165, v165\n\t"
      "v_exp_f32 v166, v166\n\t"
      "v_exp_f32 v167, v167\n\t"
      "v_pk_add_f32 v[160:161], v[160:161], s[82:83]\n\t"
      "v_pk_add_f32 v[162:163], v[162:163], s[82:83]\n\t"
      "v_pk_add_f32 v[164:165], v[164:165], s[82:83]\n\t"
      "v_pk_add_f32 v[166:167], v[166:167], s[82:83]\n\t"
      "v_rcp_f32 v160, v160\n\t"
      "v_rcp_f32 v161, v161\n\t"
      "v_rcp_f32 v162, v162\n\t"
      "v_rcp_f32 v163, v163\n\t"
      "v_rcp_f32 v164, v164\n\t"
      "v_rcp_f32 v165, v165\n\t"
      "v_rcp_f32 v166, v166\n\t"
      "v_rcp_f32 v167, v167\n\t"
      "v_pk_mul_f32 v[160:161], v[160:161], v[96:97]\n\t"
      "v_pk_mul_f32 v[162:163], v[162:163], v[98:99]\n\t"
      "v_pk_mul_f32 v[164:165], v[164:165], v[112:113]\n\t"
      "v_pk_mul_f32 v[166:167], v[166:167], v[114:115]\n\t"
      "v_pk_mul_f32 v[160:161], v[160:161], v[128:129]\n\t"
      "v_pk_mul_f32 v[162:163], v[162:163], v[130:131]\n\t"
      "v_pk_mul_f32 v[164:165], v[164:165], v[144:145]\n\t"
      "v_pk_mul_f32 v[166:167], v[166:167], v[146:147]\n\t"
      "v_cvt_pk_bf16_f32 v168, v160, v161\n\t"
      "v_cvt_pk_bf16_f32 v169, v162, v163\n\t"
      "v_cvt_pk_bf16_f32 v170, v164, v165\n\t"
      "v_cvt_pk_bf16_f32 v171, v166, v167\n\t"
      "v_pk_mul_f32 v[160:161], v[100:101], s[80:81]\n\t"
      "v_pk_mul_f32 v[162:163], v[102:103], s[80:81]\n\t"
      "v_pk_mul_f32 v[164:165], v[116:117], s[80:81]\n\t"
      "v_pk_mul_f32 v[166:167], v[118:119], s[80:81]\n\t"
      "v_exp_f32 v160, v160\n\t"
      "v_exp_f32 v161, v161\n\t"
      "v_exp_f32 v162, v162\n\t"
      "v_exp_f32 v163, v163\n\t"
      "v_exp_f32 v164, v164\n\t"
      "v_exp_f32 v165, v165\n\t"
      "v_exp_f32 v166, v166\n\t"
      "v_exp_f32 v167, v167\n\t"
      "v_pk_add_f32 v[160:161], v[160:161], s[82:83]\n\t"
      "v_pk_add_f32 v[162:163], v[162:163], s[82:83]\n\t"
      "v_pk_add_f32 v[164:165], v[164:165], s[82:83]\n\t"
      "v_pk_add_f32 v[166:167], v[166:167], s[82:83]\n\t"
      "v_rcp_f32 v160, v160\n\t"
      "v_rcp_f32 v161, v161\n\t"
      "v_rcp_f32 v162, v162\n\t"
      "v_rcp_f32 v163, v163\n\t"
      "v_rcp_f32 v164, v164\n\t"
      "v_rcp_f32 v165, v165\n\t"
      "v_rcp_f32 v166, v166\n\t"
      "v_rcp_f32 v167, v167\n\t"
      "v_pk_mul_f32 v[160:161], v[160:161], v[100:101]\n\t"
      "v_pk_mul_f32 v[162:163], v[162:163], v[102:103]\n\t"
      "v_pk_mul_f32 v[164:165], v[164:165], v[116:117]\n\t"
      "v_pk_mul_f32 v[166:167], v[166:167], v[118:119]\n\t"
      "v_pk_mul_f32 v[160:161], v[160:161], v[132:133]\n\t"
      "v_pk_mul_f32 v[162:163], v[162:163], v[134:135]\n\t"
      "v_pk_mul_f32 v[164:165], v[164:165], v[148:149]\n\t"
      "v_pk_mul_f32 v[166:167], v[166:167], v[150:151]\n\t"
      "v_cvt_pk_bf16_f32 v172, v160, v161\n\t"
      "v_cvt_pk_bf16_f32 v173, v162, v163\n\t"
      "v_cvt_pk_bf16_f32 v174, v164, v165\n\t"
      "v_cvt_pk_bf16_f32 v175, v166, v167\n\t"
      "v_pk_mul_f32 v[160:161], v[104:105], s[80:81]\n\t"
      "v_pk_mul_f32 v[162:163], v[106:107], s[80:81]\n\t"
      "v_pk_mul_f32 v[164:165], v[120:121], s[80:81]\n\t"
      "v_pk_mul_f32 v[166:167], v[122:123], s[80:81]\n\t"
      "v_exp_f32 v160, v160\n\t"
      "v_exp_f32 v161, v161\n\t"
      "v_exp_f32 v162, v162\n\t"
      "v_exp_f32 v163, v163\n\t"
      "v_exp_f32 v164, v164\n\t"
      "v_exp_f32 v165, v165\n\t"
      "v_exp_f32 v166, v166\n\t"
      "v_exp_f32 v167, v167\n\t"
      "v_pk_add_f32 v[160:161], v[160:161], s[82:83]\n\t"
      "v_pk_add_f32 v[162:163], v[162:163], s[82:83]\n\t"
      "v_pk_add_f32 v[164:165], v[164:165], s[82:83]\n\t"
      "v_pk_add_f32 v[166:167], v[166:167], s[82:83]\n\t"
      "v_rcp_f32 v160, v160\n\t"
      "v_rcp_f32 v161, v161\n\t"
      "v_rcp_f32 v162, v162\n\t"
      "v_rcp_f32 v163, v163\n\t"
      "v_rcp_f32 v164, v164\n\t"
      "v_rcp_f32 v165, v165\n\t"
      "v_rcp_f32 v166, v166\n\t"
      "v_rcp_f32 v167, v167\n\t"
      "v_pk_mul_f32 v[160:161], v[160:161], v[104:105]\n\t"
      "v_pk_mul_f32 v[162:163], v[162:163], v[106:107]\n\t"
      "v_pk_mul_f32 v[164:165], v[164:165], v[120:121]\n\t"
      "v_pk_mul_f32 v[166:167], v[166:167], v[122:123]\n\t"
      "v_pk_mul_f32 v[160:161], v[160:161], v[136:137]\n\t"
      "v_pk_mul_f32 v[162:163], v[162:163], v[138:139]\n\t"
      "v_pk_mul_f32 v[164:165], v[164:165], v[152:153]\n\t"
      "v_pk_mul_f32 v[166:167], v[166:167], v[154:155]\n\t"
      "v_cvt_pk_bf16_f32 v176, v160, v161\n\t"
      "v_cvt_pk_bf16_f32 v177, v162, v163\n\t"
      "v_cvt_pk_bf16_f32 v178, v164, v165\n\t"
      "v_cvt_pk_bf16_f32 v179, v166, v167\n\t"
      "v_pk_mul_f32 v[160:161], v[108:109], s[80:81]\n\t"
      "v_pk_mul_f32 v[162:163], v[110:111], s[80:81]\n\t"
      "v_pk_mul_f32 v[164:165], v[124:125], s[80:81]\n\t"
      "v_pk_mul_f32 v[166:167], v[126:127], s[80:81]\n\t"
      "v_exp_f32 v160, v160\n\t"
      "v_exp_f32 v161, v161\n\t"
      "v_exp_f32 v162, v162\n\t"
      "v_exp_f32 v163, v163\n\t"
      "v_exp_f32 v164, v164\n\t"
      "v_exp_f32 v165, v165\n\t"
      "v_exp_f32 v166, v166\n\t"
      "v_exp_f32 v167, v167\n\t"
      "v_pk_add_f32 v[160:161], v[160:161], s[82:83]\n\t"
      "v_pk_add_f32 v[162:163], v[162:163], s[82:83]\n\t"
      "v_pk_add_f32 v[164:165], v[164:165], s[82:83]\n\t"
      "v_pk_add_f32 v[166:167], v[166:167], s[82:83]\n\t"
      "v_rcp_f32 v160, v160\n\t"
      "v_rcp_f32 v161, v161\n\t"
      "v_rcp_f32 v162, v162\n\t"
      "v_rcp_f32 v163, v163\n\t"
      "v_rcp_f32 v164, v164\n\t"
      "v_rcp_f32 v165, v165\n\t"
      "v_rcp_f32 v166, v166\n\t"
      "v_rcp_f32 v167, v167\n\t"
      "v_pk_mul_f32 v[160:161], v[160:161], v[108:109]\n\t"
      "v_pk_mul_f32 v[162:163], v[162:163], v[110:111]\n\t"
      "v_pk_mul_f32 v[164:165], v[164:165], v[124:125]\n\t"
      "v_pk_mul_f32 v[166:167], v[166:167], v[126:127]\n\t"
      "v_pk_mul_f32 v[160:161], v[160:161], v[140:141]\n\t"
      "v_pk_mul_f32 v[162:163], v[162:163], v[142:143]\n\t"
      "v_pk_mul_f32 v[164:165], v[164:165], v[156:157]\n\t"
      "v_pk_mul_f32 v[166:167], v[166:167], v[158:159]\n\t"
      "v_cvt_pk_bf16_f32 v180, v160, v161\n\t"
      "v_cvt_pk_bf16_f32 v181, v162, v163\n\t"
      "v_cvt_pk_bf16_f32 v182, v164, v165\n\t"
      "v_cvt_pk_bf16_f32 v183, v166, v167\n\t"

      "s_mov_b64 exec, 0xffff\n\t"
      "buffer_store_dwordx2 v[12:13], v9, s[56:59], s11 offen sc1\n\t"
      "s_mov_b64 exec, -1\n\t"
      "buffer_store_dwordx4 v[168:171], v3, s[56:59], s6 offen sc1\n\t"
      "s_add_u32 s6, s6, 114688\n\t"
      "buffer_store_dwordx4 v[172:175], v3, s[56:59], s6 offen sc1\n\t"
      "s_add_u32 s6, s6, 114688\n\t"
      "buffer_store_dwordx4 v[176:179], v3, s[56:59], s6 offen sc1\n\t"
      "s_add_u32 s6, s6, 114688\n\t"
      "buffer_store_dwordx4 v[180:183], v3, s[56:59], s6 offen sc1\n\t"
      ".L_CJOIN_%=:\n\t"
      "s_barrier\n\t"
      "s_mov_b64 exec, 0x7f\n\t"
      "buffer_load_dwordx2 v[16:17], v14, s[56:59], 0 offen sc1\n\t"
      "s_mov_b64 exec, -1\n\t"
      "ds_read_b64_tr_b16 v[24:25], v252 offset:0\n\t"
      "ds_read_b64_tr_b16 v[26:27], v252 offset:2048\n\t"
      "ds_read_b64_tr_b16 v[28:29], v253 offset:0\n\t"
      "ds_read_b64_tr_b16 v[30:31], v253 offset:2048\n\t"
      "ds_read_b64_tr_b16 v[32:33], v254 offset:0\n\t"
      "ds_read_b64_tr_b16 v[34:35], v254 offset:2048\n\t"
      "ds_read_b64_tr_b16 v[36:37], v255 offset:0\n\t"
      "ds_read_b64_tr_b16 v[38:39], v255 offset:2048\n\t"
      "ds_read_b64_tr_b16 v[40:41], v252 offset:4096\n\t"
      "ds_read_b64_tr_b16 v[42:43], v252 offset:6144\n\t"
      "ds_read_b64_tr_b16 v[44:45], v253 offset:4096\n\t"
      "ds_read_b64_tr_b16 v[46:47], v253 offset:6144\n\t"
      "ds_read_b64_tr_b16 v[48:49], v254 offset:4096\n\t"
      "ds_read_b64_tr_b16 v[50:51], v254 offset:6144\n\t"
      "ds_read_b64_tr_b16 v[52:53], v255 offset:4096\n\t"
      "ds_read_b64_tr_b16 v[54:55], v255 offset:6144\n\t"
      "ds_read_b64_tr_b16 v[56:57], v252 offset:8192\n\t"
      "ds_read_b64_tr_b16 v[58:59], v252 offset:10240\n\t"
      "ds_read_b64_tr_b16 v[60:61], v253 offset:8192\n\t"
      "ds_read_b64_tr_b16 v[62:63], v253 offset:10240\n\t"
      "ds_read_b64_tr_b16 v[64:65], v254 offset:8192\n\t"
      "ds_read_b64_tr_b16 v[66:67], v254 offset:10240\n\t"
      "ds_read_b64_tr_b16 v[68:69], v255 offset:8192\n\t"
      "ds_read_b64_tr_b16 v[70:71], v255 offset:10240\n\t"
      "ds_read_b64_tr_b16 v[72:73], v252 offset:12288\n\t"
      "ds_read_b64_tr_b16 v[74:75], v252 offset:14336\n\t"
      "ds_read_b64_tr_b16 v[76:77], v253 offset:12288\n\t"
      "ds_read_b64_tr_b16 v[78:79], v253 offset:14336\n\t"
      "ds_read_b64_tr_b16 v[80:81], v254 offset:12288\n\t"
      "ds_read_b64_tr_b16 v[82:83], v254 offset:14336\n\t"
      "ds_read_b64_tr_b16 v[84:85], v255 offset:12288\n\t"
      "ds_read_b64_tr_b16 v[86:87], v255 offset:14336\n\t"
      "ds_read_b64_tr_b16 v[88:89], v252 offset:16384\n\t"
      "ds_read_b64_tr_b16 v[90:91], v252 offset:18432\n\t"
      "ds_read_b64_tr_b16 v[92:93], v253 offset:16384\n\t"
      "ds_read_b64_tr_b16 v[94:95], v253 offset:18432\n\t"
      "ds_read_b64_tr_b16 v[96:97], v254 offset:16384\n\t"
      "ds_read_b64_tr_b16 v[98:99], v254 offset:18432\n\t"
      "ds_read_b64_tr_b16 v[100:101], v255 offset:16384\n\t"
      "ds_read_b64_tr_b16 v[102:103], v255 offset:18432\n\t"
      "ds_read_b64_tr_b16 v[104:105], v252 offset:20480\n\t"
      "ds_read_b64_tr_b16 v[106:107], v252 offset:22528\n\t"
      "ds_read_b64_tr_b16 v[108:109], v253 offset:20480\n\t"
      "ds_read_b64_tr_b16 v[110:111], v253 offset:22528\n\t"
      "ds_read_b64_tr_b16 v[112:113], v254 offset:20480\n\t"
      "ds_read_b64_tr_b16 v[114:115], v254 offset:22528\n\t"
      "ds_read_b64_tr_b16 v[116:117], v255 offset:20480\n\t"
      "ds_read_b64_tr_b16 v[118:119], v255 offset:22528\n\t"
      "ds_read_b64_tr_b16 v[120:121], v244 offset:0\n\t"
      "ds_read_b64_tr_b16 v[122:123], v244 offset:2048\n\t"
      "ds_read_b64_tr_b16 v[124:125], v245 offset:0\n\t"
      "ds_read_b64_tr_b16 v[126:127], v245 offset:2048\n\t"
      "ds_read_b64_tr_b16 v[128:129], v246 offset:0\n\t"
      "ds_read_b64_tr_b16 v[130:131], v246 offset:2048\n\t"
      "ds_read_b64_tr_b16 v[132:133], v247 offset:0\n\t"
      "ds_read_b64_tr_b16 v[134:135], v247 offset:2048\n\t"

      ".L_PLOOP_%=:\n\t"
      "s_mov_b64 exec, 0x7f\n\t"
      "s_waitcnt vmcnt(0)\n\t"
      "v_cmp_ne_u64 vcc, v[16:17], v[12:13]\n\t"
      "s_mov_b64 exec, -1\n\t"
      "s_cbranch_vccz .L_PDONE_%=\n\t"
      "s_add_u32 s23, s23, 1\n\t"
      "s_cmp_gt_u32 s23, 0x100000\n\t"
      "s_cbranch_scc1 .L_PDONE_%=\n\t"
      "s_mov_b64 exec, 0x7f\n\t"
      "buffer_load_dwordx2 v[16:17], v14, s[56:59], 0 offen sc1\n\t"
      "s_mov_b64 exec, -1\n\t"
      "s_branch .L_PLOOP_%=\n\t"
      ".L_PDONE_%=:\n\t"
      "s_setprio 0\n\t"
      "s_cmp_lg_u32 s27, 0\n\t"
      "s_cbranch_scc1 .L_NOB1_%=\n\t"
      "buffer_load_dwordx2 v[16:17], v15, s[56:59], 0 offen sc1\n\t"
      ".L_NOB1_%=:\n\t"
      "buffer_load_dwordx4 v[136:139], v3, s[56:59], s72 offen offset:0 sc1\n\t"
      "buffer_load_dwordx4 v[140:143], v3, s[56:59], s72 offen offset:1024 sc1\n\t"
      "buffer_load_dwordx4 v[144:147], v3, s[56:59], s72 offen offset:2048 sc1\n\t"
      "buffer_load_dwordx4 v[148:151], v3, s[56:59], s72 offen offset:3072 sc1\n\t"
      "buffer_load_dwordx4 v[152:155], v3, s[56:59], s73 offen offset:0 sc1\n\t"
      "buffer_load_dwordx4 v[156:159], v3, s[56:59], s73 offen offset:1024 sc1\n\t"
      "s_mov_b32 s23, 0\n\t"

      ".L_P2_%=:\n\t"
      "s_waitcnt lgkmcnt(0)\n\t"
      ".L_SP0_%=:\n\t"
      "s_waitcnt vmcnt(5)\n\t"
      "v_mfma_f32_16x16x32_bf16 v[176:179], v[24:27], v[136:139], 0\n\t"
      "v_max_u32 v10, v136, v137\n\t"
      "v_mfma_f32_16x16x32_bf16 v[180:183], v[28:31], v[136:139], 0\n\t"
      "v_max3_u32 v10, v10, v138, v139\n\t"
      "v_mfma_f32_16x16x32_bf16 v[184:187], v[32:35], v[136:139], 0\n\t"
      "v_mfma_f32_16x16x32_bf16 v[188:191], v[36:39], v[136:139], 0\n\t"
      "buffer_load_dwordx4 v[160:163], v3, s[56:59], s73 offen offset:2048 sc1\n\t"
      "s_waitcnt vmcnt(5)\n\t"
      "v_mfma_f32_16x16x32_bf16 v[188:191], v[52:55], v[140:143], v[188:191]\n\t"
      "v_max3_u32 v10, v10, v140, v141\n\t"
      "v_mfma_f32_16x16x32_bf16 v[184:187], v[48:51], v[140:143], v[184:187]\n\t"
      "v_max3_u32 v10, v10, v142, v143\n\t"
      "v_mfma_f32_16x16x32_bf16 v[180:183], v[44:47], v[140:143], v[180:183]\n\t"
      "v_mfma_f32_16x16x32_bf16 v[176:179], v[40:43], v[140:143], v[176:179]\n\t"
      "buffer_load_dwordx4 v[164:167], v3, s[56:59], s74 offen offset:0 sc1\n\t"
      "s_waitcnt vmcnt(5)\n\t"
      "v_mfma_f32_16x16x32_bf16 v[176:179], v[56:59], v[144:147], v[176:179]\n\t"
      "v_max3_u32 v10, v10, v144, v145\n\t"
      "v_mfma_f32_16x16x32_bf16 v[180:183], v[60:63], v[144:147], v[180:183]\n\t"
      "v_max3_u32 v10, v10, v146, v147\n\t"
      "v_mfma_f32_16x16x32_bf16 v[184:187], v[64:67], v[144:147], v[184:187]\n\t"
      "v_mfma_f32_16x16x32_bf16 v[188:191], v[68:71], v[144:147], v[188:191]\n\t"
      "buffer_load_dwordx4 v[168:171], v3, s[56:59], s74 offen offset:1024 sc1\n\t"
      "s_waitcnt vmcnt(5)\n\t"
      "v_mfma_f32_16x16x32_bf16 v[188:191], v[84:87], v[148:151], v[188:191]\n\t"
      "v_max3_u32 v10, v10, v148, v149\n\t"
      "v_mfma_f32_16x16x32_bf16 v[184:187], v[80:83], v[148:151], v[184:187]\n\t"
      "v_max3_u32 v10, v10, v150, v151\n\t"
      "v_mfma_f32_16x16x32_bf16 v[180:183], v[76:79], v[148:151], v[180:183]\n\t"
      "v_mfma_f32_16x16x32_bf16 v[176:179], v[72:75], v[148:151], v[176:179]\n\t"
      "buffer_load_dwordx4 v[172:175], v3, s[56:59], s74 offen offset:2048 sc1\n\t"
      "s_waitcnt vmcnt(5)\n\t"
      "v_mfma_f32_16x16x32_bf16 v[176:179], v[88:91], v[152:155], v[176:179]\n\t"
      "v_max3_u32 v10, v10, v152, v153\n\t"
      "v_mfma_f32_16x16x32_bf16 v[180:183], v[92:95], v[152:155], v[180:183]\n\t"
      "v_max3_u32 v10, v10, v154, v155\n\t"
      "v_mfma_f32_16x16x32_bf16 v[184:187], v[96:99], v[152:155], v[184:187]\n\t"
      "v_mfma_f32_16x16x32_bf16 v[188:191], v[100:103], v[152:155], v[188:191]\n\t"
      "buffer_load_dwordx4 v[136:139], v3, s[56:59], s74 offen offset:3072 sc1\n\t"
      "s_waitcnt vmcnt(5)\n\t"
      "v_mfma_f32_16x16x32_bf16 v[188:191], v[116:119], v[156:159], v[188:191]\n\t"
      "v_max3_u32 v10, v10, v156, v157\n\t"
      "v_mfma_f32_16x16x32_bf16 v[184:187], v[112:115], v[156:159], v[184:187]\n\t"
      "v_max3_u32 v10, v10, v158, v159\n\t"
      "v_mfma_f32_16x16x32_bf16 v[180:183], v[108:111], v[156:159], v[180:183]\n\t"
      "v_mfma_f32_16x16x32_bf16 v[176:179], v[104:107], v[156:159], v[176:179]\n\t"
      "buffer_load_dwordx4 v[140:143], v3, s[56:59], s75 offen offset:0 sc1\n\t"
      "s_waitcnt vmcnt(5)\n\t"
      "v_mfma_f32_16x16x32_bf16 v[176:179], v[120:123], v[160:163], v[176:179]\n\t"
      "v_max3_u32 v10, v10, v160, v161\n\t"
      "v_mfma_f32_16x16x32_bf16 v[180:183], v[124:127], v[160:163], v[180:183]\n\t"
      "v_max3_u32 v10, v10, v162, v163\n\t"
      "v_mfma_f32_16x16x32_bf16 v[184:187], v[128:131], v[160:163], v[184:187]\n\t"
      "v_cmp_eq_u32 vcc, 0xffbfffff, v10\n\t"
      "v_mfma_f32_16x16x32_bf16 v[188:191], v[132:135], v[160:163], v[188:191]\n\t"
      "s_cbranch_vccnz .L_REDO0_%=\n\t"
      ".L_KEEP0_%=:\n\t"
      "buffer_load_dwordx4 v[144:147], v3, s[56:59], s75 offen offset:1024 sc1\n\t"
      ".L_SP1_%=:\n\t"
      "s_waitcnt vmcnt(5)\n\t"
      "v_mfma_f32_16x16x32_bf16 v[204:207], v[36:39], v[164:167], 0\n\t"
      "v_max_u32 v10, v164, v165\n\t"
      "v_mfma_f32_16x16x32_bf16 v[200:203], v[32:35], v[164:167], 0\n\t"
      "v_max3_u32 v10, v10, v166, v167\n\t"
      "v_mfma_f32_16x16x32_bf16 v[196:199], v[28:31], v[164:167], 0\n\t"
      "v_mfma_f32_16x16x32_bf16 v[192:195], v[24:27], v[164:167], 0\n\t"
      "buffer_load_dwordx4 v[148:151], v3, s[56:59], s75 offen offset:2048 sc1\n\t"
      "s_waitcnt vmcnt(5)\n\t"
      "v_mfma_f32_16x16x32_bf16 v[192:195], v[40:43], v[168:171], v[192:195]\n\t"
      "v_max3_u32 v10, v10, v168, v169\n\t"
      "v_mfma_f32_16x16x32_bf16 v[196:199], v[44:47], v[168:171], v[196:199]\n\t"
      "v_max3_u32 v10, v10, v170, v171\n\t"
      "v_mfma_f32_16x16x32_bf16 v[200:203], v[48:51], v[168:171], v[200:203]\n\t"
      "v_mfma_f32_16x16x32_bf16 v[204:207], v[52:55], v[168:171], v[204:207]\n\t"
      "buffer_load_dwordx4 v[152:155], v3, s[56:59], s76 offen offset:0 sc1\n\t"
      "s_setprio 2\n\t"
      "ds_write_b128 v22, v[176:179] offset:0\n\t"
      "ds_write_b128 v23, v[180:183] offset:0\n\t"
      "ds_write_b128 v22, v[184:187] offset:128\n\t"
      "ds_write_b128 v23, v[188:191] offset:128\n\t"
      "s_setprio 0\n\t"
      "s_waitcnt vmcnt(5)\n\t"
      "v_mfma_f32_16x16x32_bf16 v[204:207], v[68:71], v[172:175], v[204:207]\n\t"
      "v_max3_u32 v10, v10, v172, v173\n\t"
      "v_mfma_f32_16x16x32_bf16 v[200:203], v[64:67], v[172:175], v[200:203]\n\t"
      "v_max3_u32 v10, v10, v174, v175\n\t"
      "v_mfma_f32_16x16x32_bf16 v[196:199], v[60:63], v[172:175], v[196:199]\n\t"
      "v_mfma_f32_16x16x32_bf16 v[192:195], v[56:59], v[172:175], v[192:195]\n\t"
      "buffer_load_dwordx4 v[156:159], v3, s[56:59], s76 offen offset:1024 sc1\n\t"
      "s_waitcnt vmcnt(5)\n\t"
      "v_mfma_f32_16x16x32_bf16 v[192:195], v[72:75], v[136:139], v[192:195]\n\t"
      "v_max3_u32 v10, v10, v136, v137\n\t"
      "v_mfma_f32_16x16x32_bf16 v[196:199], v[76:79], v[136:139], v[196:199]\n\t"
      "v_max3_u32 v10, v10, v138, v139\n\t"
      "v_mfma_f32_16x16x32_bf16 v[200:203], v[80:83], v[136:139], v[200:203]\n\t"
      "v_mfma_f32_16x16x32_bf16 v[204:207], v[84:87], v[136:139], v[204:207]\n\t"
      "buffer_load_dwordx4 v[160:163], v3, s[56:59], s76 offen offset:2048 sc1\n\t"
      "s_waitcnt vmcnt(5)\n\t"
      "v_mfma_f32_16x16x32_bf16 v[204:207], v[100:103], v[140:143], v[204:207]\n\t"
      "v_max3_u32 v10, v10, v140, v141\n\t"
      "v_mfma_f32_16x16x32_bf16 v[200:203], v[96:99], v[140:143], v[200:203]\n\t"
      "v_max3_u32 v10, v10, v142, v143\n\t"
      "v_mfma_f32_16x16x32_bf16 v[196:199], v[92:95], v[140:143], v[196:199]\n\t"
      "v_mfma_f32_16x16x32_bf16 v[192:195], v[88:91], v[140:143], v[192:195]\n\t"
      "buffer_load_dwordx4 v[164:167], v3, s[56:59], s76 offen offset:3072 sc1\n\t"
      "s_waitcnt vmcnt(5)\n\t"
      "v_mfma_f32_16x16x32_bf16 v[192:195], v[104:107], v[144:147], v[192:195]\n\t"
      "v_max3_u32 v10, v10, v144, v145\n\t"
      "v_mfma_f32_16x16x32_bf16 v[196:199], v[108:111], v[144:147], v[196:199]\n\t"
      "v_max3_u32 v10, v10, v146, v147\n\t"
      "v_mfma_f32_16x16x32_bf16 v[200:203], v[112:115], v[144:147], v[200:203]\n\t"
      "v_mfma_f32_16x16x32_bf16 v[204:207], v[116:119], v[144:147], v[204:207]\n\t"
      "buffer_load_dwordx4 v[168:171], v3, s[56:59], s77 offen offset:0 sc1\n\t"
      "s_waitcnt vmcnt(5)\n\t"
      "v_mfma_f32_16x16x32_bf16 v[204:207], v[132:135], v[148:151], v[204:207]\n\t"
      "v_max3_u32 v10, v10, v148, v149\n\t"
      "v_mfma_f32_16x16x32_bf16 v[200:203], v[128:131], v[148:151], v[200:203]\n\t"
      "v_max3_u32 v10, v10, v150, v151\n\t"
      "v_mfma_f32_16x16x32_bf16 v[196:199], v[124:127], v[148:151], v[196:199]\n\t"
      "v_cmp_eq_u32 vcc, 0xffbfffff, v10\n\t"
      "v_mfma_f32_16x16x32_bf16 v[192:195], v[120:123], v[148:151], v[192:195]\n\t"
      "s_cbranch_vccnz .L_REDO1_%=\n\t"
      ".L_KEEP1_%=:\n\t"
      "buffer_load_dwordx4 v[172:175], v3, s[56:59], s77 offen offset:1024 sc1\n\t"
      ".L_SP2_%=:\n\t"
      "s_waitcnt vmcnt(5)\n\t"
      "v_mfma_f32_16x16x32_bf16 v[176:179], v[24:27], v[152:155], 0\n\t"
      "v_max_u32 v10, v152, v153\n\t"
      "v_mfma_f32_16x16x32_bf16 v[180:183], v[28:31], v[152:155], 0\n\t"
      "v_max3_u32 v10, v10, v154, v155\n\t"
      "v_mfma_f32_16x16x32_bf16 v[184:187], v[32:35], v[152:155], 0\n\t"
      "v_mfma_f32_16x16x32_bf16 v[188:191], v[36:39], v[152:155], 0\n\t"
      "buffer_load_dwordx4 v[136:139], v3, s[56:59], s77 offen offset:2048 sc1\n\t"
      "s_waitcnt vmcnt(5)\n\t"
      "v_mfma_f32_16x16x32_bf16 v[188:191], v[52:55], v[156:159], v[188:191]\n\t"
      "v_max3_u32 v10, v10, v156, v157\n\t"
      "v_mfma_f32_16x16x32_bf16 v[184:187], v[48:51], v[156:159], v[184:187]\n\t"
      "v_max3_u32 v10, v10, v158, v159\n\t"
      "v_mfma_f32_16x16x32_bf16 v[180:183], v[44:47], v[156:159], v[180:183]\n\t"
      "v_mfma_f32_16x16x32_bf16 v[176:179], v[40:43], v[156:159], v[176:179]\n\t"
      "buffer_load_dwordx4 v[140:143], v3, s[56:59], s78 offen offset:0 sc1\n\t"
      "s_setprio 2\n\t"
      "ds_write_b128 v22, v[192:195] offset:4096\n\t"
      "ds_write_b128 v23, v[196:199] offset:4096\n\t"
      "ds_write_b128 v22, v[200:203] offset:4224\n\t"
      "ds_write_b128 v23, v[204:207] offset:4224\n\t"
      "s_setprio 0\n\t"
      "s_waitcnt vmcnt(5)\n\t"
      "v_mfma_f32_16x16x32_bf16 v[176:179], v[56:59], v[160:163], v[176:179]\n\t"
      "v_max3_u32 v10, v10, v160, v161\n\t"
      "v_mfma_f32_16x16x32_bf16 v[180:183], v[60:63], v[160:163], v[180:183]\n\t"
      "v_max3_u32 v10, v10, v162, v163\n\t"
      "v_mfma_f32_16x16x32_bf16 v[184:187], v[64:67], v[160:163], v[184:187]\n\t"
      "v_mfma_f32_16x16x32_bf16 v[188:191], v[68:71], v[160:163], v[188:191]\n\t"
      "buffer_load_dwordx4 v[144:147], v3, s[56:59], s78 offen offset:1024 sc1\n\t"
      "s_waitcnt vmcnt(5)\n\t"
      "v_mfma_f32_16x16x32_bf16 v[188:191], v[84:87], v[164:167], v[188:191]\n\t"
      "v_max3_u32 v10, v10, v164, v165\n\t"
      "v_mfma_f32_16x16x32_bf16 v[184:187], v[80:83], v[164:167], v[184:187]\n\t"
      "v_max3_u32 v10, v10, v166, v167\n\t"
      "v_mfma_f32_16x16x32_bf16 v[180:183], v[76:79], v[164:167], v[180:183]\n\t"
      "v_mfma_f32_16x16x32_bf16 v[176:179], v[72:75], v[164:167], v[176:179]\n\t"
      "buffer_load_dwordx4 v[148:151], v3, s[56:59], s78 offen offset:2048 sc1\n\t"
      "buffer_load_dwordx4 v[152:155], v3, s[56:59], s78 offen offset:3072 sc1\n\t"
      "s_cmp_lg_u32 s22, 0\n\t"
      "s_mov_b32 s22, 1\n\t"
      "s_waitcnt lgkmcnt(0)\n\t"
      "s_cbranch_scc1 .L_NOX_%=\n\t"
      "s_barrier\n\t"
      ".L_NOX_%=:\n\t"
      "ds_read_b128 v[208:211], v248 offset:0\n\t"
      "ds_read_b128 v[212:215], v248 offset:24576\n\t"
      "ds_read_b128 v[216:219], v248 offset:49152\n\t"
      "ds_read_b128 v[220:223], v249 offset:0\n\t"
      "ds_read_b128 v[224:227], v249 offset:24576\n\t"
      "ds_read_b128 v[228:231], v249 offset:40960\n\t"
      "ds_read_b128 v[232:235], v250 offset:0\n\t"
      "ds_read_b128 v[236:239], v250 offset:16384\n\t"
      "s_waitcnt vmcnt(6)\n\t"
      "v_mfma_f32_16x16x32_bf16 v[176:179], v[88:91], v[168:171], v[176:179]\n\t"
      "v_max3_u32 v10, v10, v168, v169\n\t"
      "v_mfma_f32_16x16x32_bf16 v[180:183], v[92:95], v[168:171], v[180:183]\n\t"
      "v_max3_u32 v10, v10, v170, v171\n\t"
      "v_mfma_f32_16x16x32_bf16 v[184:187], v[96:99], v[168:171], v[184:187]\n\t"
      "v_mfma_f32_16x16x32_bf16 v[188:191], v[100:103], v[168:171], v[188:191]\n\t"
      "buffer_load_dwordx4 v[156:159], v3, s[56:59], s79 offen offset:0 sc1\n\t"
      "buffer_load_dwordx4 v[160:163], v3, s[56:59], s79 offen offset:1024 sc1\n\t"
      "s_waitcnt vmcnt(7)\n\t"
      "v_mfma_f32_16x16x32_bf16 v[188:191], v[116:119], v[172:175], v[188:191]\n\t"
      "v_max3_u32 v10, v10, v172, v173\n\t"
      "v_mfma_f32_16x16x32_bf16 v[184:187], v[112:115], v[172:175], v[184:187]\n\t"
      "v_max3_u32 v10, v10, v174, v175\n\t"
      "v_mfma_f32_16x16x32_bf16 v[180:183], v[108:111], v[172:175], v[180:183]\n\t"
      "v_mfma_f32_16x16x32_bf16 v[176:179], v[104:107], v[172:175], v[176:179]\n\t"
      "buffer_load_dwordx4 v[164:167], v3, s[56:59], s79 offen offset:2048 sc1\n\t"
      "s_waitcnt lgkmcnt(0)\n\t"
      "v_pk_add_f32 v[208:209], v[208:209], v[212:213]\n\t"
      "v_pk_add_f32 v[210:211], v[210:211], v[214:215]\n\t"
      "v_pk_add_f32 v[208:209], v[208:209], v[216:217]\n\t"
      "v_pk_add_f32 v[210:211], v[210:211], v[218:219]\n\t"
      "v_pk_add_f32 v[208:209], v[208:209], v[220:221]\n\t"
      "v_pk_add_f32 v[210:211], v[210:211], v[222:223]\n\t"
      "v_pk_add_f32 v[208:209], v[208:209], v[224:225]\n\t"
      "v_pk_add_f32 v[210:211], v[210:211], v[226:227]\n\t"
      "v_pk_add_f32 v[208:209], v[208:209], v[228:229]\n\t"
      "v_pk_add_f32 v[210:211], v[210:211], v[230:231]\n\t"
      "v_pk_add_f32 v[208:209], v[208:209], v[232:233]\n\t"
      "v_pk_add_f32 v[210:211], v[210:211], v[234:235]\n\t"
      "v_pk_add_f32 v[208:209], v[208:209], v[236:237]\n\t"
      "v_pk_add_f32 v[210:211], v[210:211], v[238:239]\n\t"
      "v_bfi_b32 v208, s4, v12, v208\n\t"
      "v_bfi_b32 v209, s4, v13, v209\n\t"
      "v_bfi_b32 v210, s4, v12, v210\n\t"
      "v_bfi_b32 v211, s4, v13, v211\n\t"
      "buffer_store_dwordx4 v[208:211], v3, s[56:59], s95 offen sc1\n\t"
      "s_cmp_eq_u32 s27, 0\n\t"
      "s_cbranch_scc1 .L_BUMP_%=\n\t"
      ".L_NOB2_%=:\n\t"
      "s_waitcnt vmcnt(8)\n\t"
      "v_mfma_f32_16x16x32_bf16 v[176:179], v[120:123], v[136:139], v[176:179]\n\t"
      "v_max3_u32 v10, v10, v136, v137\n\t"
      "v_mfma_f32_16x16x32_bf16 v[180:183], v[124:127], v[136:139], v[180:183]\n\t"
      "v_max3_u32 v10, v10, v138, v139\n\t"
      "v_mfma_f32_16x16x32_bf16 v[184:187], v[128:131], v[136:139], v[184:187]\n\t"
      "v_cmp_eq_u32 vcc, 0xffbfffff, v10\n\t"
      "v_mfma_f32_16x16x32_bf16 v[188:191], v[132:135], v[136:139], v[188:191]\n\t"
      "s_cbranch_vccnz .L_REDO2_%=\n\t"
      ".L_KEEP2_%=:\n\t"
      ".L_SP3_%=:\n\t"
      "s_waitcnt vmcnt(7)\n\t"
      "v_mfma_f32_16x16x32_bf16 v[204:207], v[36:39], v[140:143], 0\n\t"
      "v_max_u32 v10, v140, v141\n\t"
      "v_mfma_f32_16x16x32_bf16 v[200:203], v[32:35], v[140:143], 0\n\t"
      "v_max3_u32 v10, v10, v142, v143\n\t"
      "v_mfma_f32_16x16x32_bf16 v[196:199], v[28:31], v[140:143], 0\n\t"
      "v_mfma_f32_16x16x32_bf16 v[192:195], v[24:27], v[140:143], 0\n\t"
      "s_waitcnt vmcnt(6)\n\t"
      "v_mfma_f32_16x16x32_bf16 v[192:195], v[40:43], v[144:147], v[192:195]\n\t"
      "v_max3_u32 v10, v10, v144, v145\n\t"
      "v_mfma_f32_16x16x32_bf16 v[196:199], v[44:47], v[144:147], v[196:199]\n\t"
      "v_max3_u32 v10, v10, v146, v147\n\t"
      "v_mfma_f32_16x16x32_bf16 v[200:203], v[48:51], v[144:147], v[200:203]\n\t"
      "v_mfma_f32_16x16x32_bf16 v[204:207], v[52:55], v[144:147], v[204:207]\n\t"
      "s_setprio 2\n\t"
      "ds_write_b128 v22, v[176:179] offset:8192\n\t"
      "ds_write_b128 v23, v[180:183] offset:8192\n\t"
      "ds_write_b128 v22, v[184:187] offset:8320\n\t"
      "ds_write_b128 v23, v[188:191] offset:8320\n\t"
      "s_setprio 0\n\t"
      "s_waitcnt vmcnt(5)\n\t"
      "v_mfma_f32_16x16x32_bf16 v[204:207], v[68:71], v[148:151], v[204:207]\n\t"
      "v_max3_u32 v10, v10, v148, v149\n\t"
      "v_mfma_f32_16x16x32_bf16 v[200:203], v[64:67], v[148:151], v[200:203]\n\t"
      "v_max3_u32 v10, v10, v150, v151\n\t"
      "v_mfma_f32_16x16x32_bf16 v[196:199], v[60:63], v[148:151], v[196:199]\n\t"
      "v_mfma_f32_16x16x32_bf16 v[192:195], v[56:59], v[148:151], v[192:195]\n\t"
      "s_waitcnt vmcnt(4)\n\t"
      "v_mfma_f32_16x16x32_bf16 v[192:195], v[72:75], v[152:155], v[192:195]\n\t"
      "v_max3_u32 v10, v10, v152, v153\n\t"
      "v_mfma_f32_16x16x32_bf16 v[196:199], v[76:79], v[152:155], v[196:199]\n\t"
      "v_max3_u32 v10, v10, v154, v155\n\t"
      "v_mfma_f32_16x16x32_bf16 v[200:203], v[80:83], v[152:155], v[200:203]\n\t"
      "v_mfma_f32_16x16x32_bf16 v[204:207], v[84:87], v[152:155], v[204:207]\n\t"
      "buffer_load_dwordx4 v[240:243], v3, s[56:59], s94 offen sc1\n\t"
      "s_waitcnt vmcnt(4)\n\t"
      "v_mfma_f32_16x16x32_bf16 v[204:207], v[100:103], v[156:159], v[204:207]\n\t"
      "v_max3_u32 v10, v10, v156, v157\n\t"
      "v_mfma_f32_16x16x32_bf16 v[200:203], v[96:99], v[156:159], v[200:203]\n\t"
      "v_max3_u32 v10, v10, v158, v159\n\t"
      "v_mfma_f32_16x16x32_bf16 v[196:199], v[92:95], v[156:159], v[196:199]\n\t"
      "v_mfma_f32_16x16x32_bf16 v[192:195], v[88:91], v[156:159], v[192:195]\n\t"
      "s_waitcnt vmcnt(3)\n\t"
      "v_mfma_f32_16x16x32_bf16 v[192:195], v[104:107], v[160:163], v[192:195]\n\t"
      "v_max3_u32 v10, v10, v160, v161\n\t"
      "v_mfma_f32_16x16x32_bf16 v[196:199], v[108:111], v[160:163], v[196:199]\n\t"
      "v_max3_u32 v10, v10, v162, v163\n\t"
      "v_mfma_f32_16x16x32_bf16 v[200:203], v[112:115], v[160:163], v[200:203]\n\t"
      "v_mfma_f32_16x16x32_bf16 v[204:207], v[116:119], v[160:163], v[204:207]\n\t"
      "s_waitcnt vmcnt(2)\n\t"
      "v_mfma_f32_16x16x32_bf16 v[204:207], v[132:135], v[164:167], v[204:207]\n\t"
      "v_max3_u32 v10, v10, v164, v165\n\t"
      "v_mfma_f32_16x16x32_bf16 v[200:203], v[128:131], v[164:167], v[200:203]\n\t"
      "v_max3_u32 v10, v10, v166, v167\n\t"
      "v_mfma_f32_16x16x32_bf16 v[196:199], v[124:127], v[164:167], v[196:199]\n\t"
      "v_cmp_eq_u32 vcc, 0xffbfffff, v10\n\t"
      "v_mfma_f32_16x16x32_bf16 v[192:195], v[120:123], v[164:167], v[192:195]\n\t"
      "s_cbranch_vccnz .L_REDO3_%=\n\t"
      ".L_KEEP3_%=:\n\t"
      "v_and_b32 v6, s4, v12\n\t"
      "v_and_b32 v7, s4, v13\n\t"
      "s_setprio 2\n\t"
      "s_nop 1\n\t"
      "ds_write_b128 v23, v[204:207] offset:12416\n\t"
      "ds_write_b128 v22, v[200:203] offset:12416\n\t"
      "ds_write_b128 v23, v[196:199] offset:12288\n\t"
      "ds_write_b128 v22, v[192:195] offset:12288\n\t"
      "s_waitcnt lgkmcnt(0)\n\t"
      "s_barrier\n\t"
      "ds_read_b128 v[208:211], v248 offset:8192\n\t"
      "ds_read_b128 v[212:215], v248 offset:32768\n\t"
      "ds_read_b128 v[216:219], v248 offset:57344\n\t"
      "ds_read_b128 v[220:223], v249 offset:8192\n\t"
      "ds_read_b128 v[224:227], v249 offset:32768\n\t"
      "ds_read_b128 v[228:231], v249 offset:49152\n\t"
      "ds_read_b128 v[232:235], v250 offset:8192\n\t"
      "ds_read_b128 v[236:239], v250 offset:24576\n\t"
      ".L_INB_%=:\n\t"
      "s_waitcnt vmcnt(0)\n\t"
      "v_xor_b32 v240, v240, v6\n\t"
      "v_xor_b32 v241, v241, v7\n\t"
      "v_xor_b32 v242, v242, v6\n\t"
      "v_xor_b32 v243, v243, v7\n\t"
      "v_or3_b32 v4, v240, v241, v242\n\t"
      "v_bitop3_b32 v4, v4, v243, s4 bitop3:0xa8\n\t"
      "v_cmp_ne_u32 vcc, 0, v4\n\t"
      "s_cbranch_vccnz .L_INRETRY_%=\n\t"
      ".L_FIN_%=:\n\t"
      "s_waitcnt lgkmcnt(6)\n\t"
      "v_pk_add_f32 v[208:209], v[208:209], v[240:241]\n\t"
      "v_pk_add_f32 v[210:211], v[210:211], v[242:243]\n\t"
      "v_pk_add_f32 v[208:209], v[208:209], v[212:213]\n\t"
      "v_pk_add_f32 v[210:211], v[210:211], v[214:215]\n\t"
      "s_waitcnt lgkmcnt(4)\n\t"
      "v_pk_add_f32 v[216:217], v[216:217], v[220:221]\n\t"
      "v_pk_add_f32 v[218:219], v[218:219], v[222:223]\n\t"
      "v_pk_add_f32 v[208:209], v[208:209], v[216:217]\n\t"
      "v_pk_add_f32 v[210:211], v[210:211], v[218:219]\n\t"
      "s_waitcnt lgkmcnt(2)\n\t"
      "v_pk_add_f32 v[224:225], v[224:225], v[228:229]\n\t"
      "v_pk_add_f32 v[226:227], v[226:227], v[230:231]\n\t"
      "v_pk_add_f32 v[208:209], v[208:209], v[224:225]\n\t"
      "v_pk_add_f32 v[210:211], v[210:211], v[226:227]\n\t"
      "s_waitcnt lgkmcnt(0)\n\t"
      "v_pk_add_f32 v[232:233], v[232:233], v[236:237]\n\t"
      "v_pk_add_f32 v[234:235], v[234:235], v[238:239]\n\t"
      "v_pk_add_f32 v[208:209], v[208:209], v[232:233]\n\t"
      "v_pk_add_f32 v[210:211], v[210:211], v[234:235]\n\t"
      "v_cvt_pk_bf16_f32 v4, v208, v209\n\t"
      "v_cvt_pk_bf16_f32 v5, v210, v211\n\t"
      "buffer_store_dwordx2 v[4:5], v251, s[60:63], s10 offen nt\n\t"
      "s_endpgm\n\t"
      ".L_INRETRY_%=:\n\t"
      "s_add_u32 s23, s23, 1\n\t"
      "s_cmp_gt_u32 s23, 0x100000\n\t"
      "s_cbranch_scc1 .L_FIN_%=\n\t"
      "buffer_load_dwordx4 v[240:243], v3, s[56:59], s94 offen sc1\n\t"
      "s_branch .L_INB_%=\n\t"
      :
      : "s"(__builtin_amdgcn_kernarg_segment_ptr()), "s"(blockIdx.x), "v"(threadIdx.x),
        "s"((unsigned)(size_t)(__attribute__((address_space(3))) char*)lds)
      : "v1", "v2", "v3", "v4", "v5", "v6", "v7", "v8", "v9", "v10", "v11", "v12", "v13", "v14", "v15", "v16",
        "v17", "v18", "v19", "v20", "v21", "v22", "v23", "v24", "v25", "v26", "v27", "v28", "v29", "v30", "v31", "v32",
        "v33", "v34", "v35", "v36", "v37", "v38", "v39", "v40", "v41", "v42", "v43", "v44", "v45", "v46", "v47", "v48",
        "v49", "v50", "v51", "v52", "v53", "v54", "v55", "v56", "v57", "v58", "v59", "v60", "v61", "v62", "v63", "v64",
        "v65", "v66", "v67", "v68", "v69", "v70", "v71", "v72", "v73", "v74", "v75", "v76", "v77", "v78", "v79", "v80",
        "v81", "v82", "v83", "v84", "v85", "v86", "v87", "v88", "v89", "v90", "v91", "v92", "v93", "v94", "v95", "v96",
        "v97", "v98", "v99", "v100", "v101", "v102", "v103", "v104", "v105", "v106", "v107", "v108", "v109", "v110", "v111", "v112",
        "v113", "v114", "v115", "v116", "v117", "v118", "v119", "v120", "v121", "v122", "v123", "v124", "v125", "v126", "v127", "v128",
        "v129", "v130", "v131", "v132", "v133", "v134", "v135", "v136", "v137", "v138", "v139", "v140", "v141", "v142", "v143", "v144",
        "v145", "v146", "v147", "v148", "v149", "v150", "v151", "v152", "v153", "v154", "v155", "v156", "v157", "v158", "v159", "v160",
        "v161", "v162", "v163", "v164", "v165", "v166", "v167", "v168", "v169", "v170", "v171", "v172", "v173", "v174", "v175", "v176",
        "v177", "v178", "v179", "v180", "v181", "v182", "v183", "v184", "v185", "v186", "v187", "v188", "v189", "v190", "v191", "v192",
        "v193", "v194", "v195", "v196", "v197", "v198", "v199", "v200", "v201", "v202", "v203", "v204", "v205", "v206", "v207", "v208",
        "v209", "v210", "v211", "v212", "v213", "v214", "v215", "v216", "v217", "v218", "v219", "v220", "v221", "v222", "v223", "v224",
        "v225", "v226", "v227", "v228", "v229", "v230", "v231", "v232", "v233", "v234", "v235", "v236", "v237", "v238", "v239", "v240",
        "v241", "v242", "v243", "v244", "v245", "v246", "v247", "v248", "v249", "v250", "v251", "v252", "v253", "v254", "v255", "s4",
        "s5", "s6", "s7", "s8", "s9", "s10", "s11", "s12", "s13", "s14", "s15", "s16", "s17", "s18", "s19", "s20",
        "s21", "s22", "s23", "s24", "s25", "s26", "s27", "s28", "s29", "s30", "s31", "s36", "s37", "s38", "s39", "s40",
        "s41", "s42", "s43", "s44", "s45", "s46", "s47", "s48", "s49", "s50", "s51", "s52", "s53", "s54", "s55", "s56",
        "s57", "s58", "s59", "s60", "s61", "s62", "s63", "s64", "s65", "s66", "s67", "s68", "s69", "s70", "s71", "s72",
        "s73", "s74", "s75", "s76", "s77", "s78", "s79", "s80", "s81", "s82", "s83", "s84", "s85", "s86", "s87", "s88",
        "s89", "s90", "s91", "s92", "s93", "s94", "s95", "vcc", "scc", "memory");
}

static void fill_order(Ord& ord) {
  const unsigned long long tab[8] = {
      0xa2689c45b3d1e0f7ull, 0x9128ab36c504df7eull, 0x2918a0645cb3df7eull, 0x269c8ab15307f4deull,
      0x8a293456bcde10f7ull, 0x2a8c96035b14d7feull, 0x93821a6cb0547dfeull, 0x83a2619c50bf74deull};
  for (int i = 0; i < 8; ++i) ord.o[i] = tab[i];
}

extern "C" void kernel_launch(void* const* d_in, const int* in_sizes, int n_in, void* d_out, int out_size, void* d_ws,
                              size_t ws_size, hipStream_t stream) {
  Ord ord;
  fill_order(ord);
  swiglu_fused<<<256, 512, 0, stream>>>(d_in[0], d_in[1], d_in[2], d_in[3], d_out, d_ws, ord);
}
```
